```python
import torch

def _softmax(x, axis=-1):
    return torch.softmax(x, dim=axis)
def _gelu(x):
    return torch.nn.functional.gelu(x, approximate="tanh")
def _silu(x):
    return torch.nn.functional.silu(x)
def _sigmoid(x):
    return torch.sigmoid(x)
def _layernorm(x, axis=-1, eps=1e-5):
    m = x.mean(dim=axis, keepdim=True)
    v = x.var(dim=axis, keepdim=True, unbiased=False)
    return (x - m) / torch.sqrt(v + eps)
def _scatter_add(tgt, idx, src, axis=0):
    return tgt.clone().index_add_(axis, idx, src)
def _segment_sum(src, seg, n):
    out = torch.zeros((n,) + src.shape[1:], dtype=src.dtype)
    return out.index_add_(0, seg, src)
def _pad(x, pw):
    flat = tuple(v for pair in reversed(pw) for v in pair)
    return torch.nn.functional.pad(x, flat)

kernel_name = 'standalone__self_sup_conv_stem_hubert__speech'

def setup_inputs():

    _w = (lambda a: (lambda t: t.to(torch.bfloat16) if t.is_floating_point() else t)(torch.as_tensor(a)))
    B, T, D, K = 2, 4000, 512, 3
    scale = (1.0 / (K * D)) ** 0.5
    return {
        "x": _w((lambda *s: torch.randn(*s, dtype=torch.bfloat16) * 0.05)(B, T, D)),
        "w": _w((lambda *s: torch.randn(*s, dtype=torch.bfloat16) * 0.05)(K, D, D) * scale),
        "b": _w((lambda *s: torch.randn(*s, dtype=torch.bfloat16) * 0.05)(D) * 0.1),
        "gamma": _w(torch.ones((D,), dtype=float)),
        "beta": _w(torch.zeros((D,), dtype=float)),
    }

def reference(x, w, b, gamma, beta):
    B, T, D = x.shape
    K = w.shape[0]
    s = 2
    L = (T - K) // s + 1
    y = torch.einsum('blc,cd->bld', x[:, 0 : s * L : s, :], w[0])
    for i in range(1, K):
        y = y + torch.einsum('blc,cd->bld', x[:, i : i + s * L : s, :], w[i])
    y = y + b[None, None, :]
    mu = y.mean(1, keepdims=True)
    var = ((y - mu) ** 2).mean(1, keepdims=True)
    yn = (y - mu) / torch.sqrt(var + 1e-5)
    yn = yn * gamma[None, None, :] + beta[None, None, :]
    return _gelu(yn)
```

```cpp
#include <hip/hip_runtime.h>
#include <hip/hip_bf16.h>

typedef __attribute__((ext_vector_type(8))) short s16x8;
typedef __attribute__((ext_vector_type(4))) short s16x4;
typedef __attribute__((ext_vector_type(8))) __bf16 bf16x8;
typedef __attribute__((ext_vector_type(16))) float f32x16;
typedef __attribute__((address_space(3))) s16x4* lds_s16x4_p;
typedef __attribute__((address_space(1))) unsigned long long gu64;

constexpr int TT = 4000, DD = 512, LL = 1999;
constexpr int BM = 128, BN = 64;
constexpr int CK = 32;
constexpr int NST = DD / CK;
constexpr int NBUF = 4;
constexpr int E_OFF = 0, O_OFF = 144 * 64, W_OFF = O_OFF + 128 * 64;
constexpr int STAGE_B = W_OFF + 96 * 128;
constexpr int NPIECE = 29;
constexpr int LDS_B = NBUF * STAGE_B;

#define WAIT_V(n) asm volatile("s_waitcnt vmcnt(%0)" ::"n"(n) : "memory")

__device__ __forceinline__ void glds16(const void* g, unsigned lds) {
    asm volatile("s_mov_b32 m0, %1\n\ts_nop 0\n\tglobal_load_lds_dwordx4 %0, off" ::"v"(g), "s"(lds) : "memory", "m0");
}
template <int N> __device__ __forceinline__ void wait_vm() { WAIT_V(N); }

__device__ __forceinline__ void chan_merge(float& n, float& m, float& q, float nb, float mb, float qb) {
    float nt = n + nb;
    if (nb > 0.f) {
        float d = mb - m;
        float r = nb / nt;
        m = m + d * r;
        q = q + qb + d * d * n * r;
        n = nt;
    }
}

__global__ __launch_bounds__(256, 1)
void conv_gn_gelu_kernel(const __hip_bfloat16* __restrict__ x, const __hip_bfloat16* __restrict__ w,
                         const __hip_bfloat16* __restrict__ bias, const __hip_bfloat16* __restrict__ gamma,
                         const __hip_bfloat16* __restrict__ beta, __hip_bfloat16* __restrict__ out,
                         unsigned long long* __restrict__ gran) {
    __shared__ __attribute__((aligned(16))) char smem[LDS_B];

    const int tid = threadIdx.x;
    const int lane = tid & 63;
    const int wv = __builtin_amdgcn_readfirstlane(tid >> 6);
    const int wr = wv >> 1, wc = wv & 1;

    const int bid = blockIdx.x;
    const int xcd = bid & 7, idx = bid >> 3;
    const int mt = xcd * 4 + (idx >> 3);
    const int nt = idx & 7;
    const int batch = mt >> 4, mtb = mt & 15;
    const int l0 = mtb * BM, n0 = nt * BN;

    const char* xb = (const char*)(x + (size_t)batch * TT * DD);
    const char* srcp[8];
#pragma unroll
    for (int j = 0; j < 8; ++j) {
        int p = wv + 4 * j;
        const char* s;
        if (p < 9) {
            int r = 16 * p + (lane >> 2);
            int c = (lane & 3) ^ ((r >> 2) & 3);
            int row = 2 * (l0 + r);
            row = row < TT ? row : TT - 1;
            s = xb + (size_t)row * (DD * 2) + c * 16;
        } else if (p < 17) {
            int r = 16 * (p - 9) + (lane >> 2);
            int c = (lane & 3) ^ ((r >> 2) & 3);
            int row = 2 * (l0 + r) + 1;
            row = row < TT ? row : TT - 1;
            s = xb + (size_t)row * (DD * 2) + c * 16;
        } else {
            int pp = p - 17;
            int kr = 8 * pp + (lane >> 3);
            int tap = kr >> 5, kk = kr & 31;
            int c = (lane & 7) ^ (((kr >> 1) & 1) << 2);
            s = (const char*)w + ((size_t)(tap * DD + kk) * DD + n0) * 2 + c * 16;
        }
        srcp[j] = s;
    }
    const unsigned lds_base = (unsigned)(size_t)(__attribute__((address_space(3))) char*)smem;
    auto issue = [&](int st, int buf) {
        unsigned lb = lds_base + buf * STAGE_B + wv * 1024;
#pragma unroll
        for (int j = 0; j < 8; ++j) {
            int p = wv + 4 * j;
            if (p < NPIECE) {
                const char* s = srcp[j] + (p < 17 ? (size_t)st * (CK * 2) : (size_t)st * (CK * DD * 2));
                glds16(s, lb + j * 4096);
            }
        }
    };

    const int h = lane >> 5;
    int aoff[3][2];
#pragma unroll
    for (int mi = 0; mi < 2; ++mi) {
        int r = wr * 64 + mi * 32 + (lane & 31);
        aoff[0][mi] = E_OFF + r * 64 + 16 * (h ^ ((r >> 2) & 3));
        aoff[1][mi] = O_OFF + r * 64 + 16 * (h ^ ((r >> 2) & 3));
        int r1 = r + 1;
        aoff[2][mi] = E_OFF + r1 * 64 + 16 * (h ^ ((r1 >> 2) & 3));
    }
    int boff;
    {
        int blk = (lane >> 4) & 1, q = (lane & 15) >> 2, p = lane & 3;
        int row = 8 * h + q;
        int ch = wc * 4 + 2 * blk + (p >> 1);
        boff = W_OFF + row * 128 + 16 * (ch ^ (((q >> 1) & 1) << 2)) + 8 * (p & 1);
    }

    f32x16 acc[2];
#pragma unroll
    for (int mi = 0; mi < 2; ++mi)
#pragma unroll
        for (int i = 0; i < 16; ++i) acc[mi][i] = 0.f;

#pragma unroll
    for (int s = 0; s < NBUF - 1; ++s) issue(s, s);

#pragma unroll
    for (int it = 0; it < NST; ++it) {
        constexpr int dummy = 0; (void)dummy;
        const int infl = (NST - 1 - it) < (NBUF - 2) ? (NST - 1 - it) : (NBUF - 2);
        if (wv == 0) {
            if (infl == 2) wait_vm<16>(); else if (infl == 1) wait_vm<8>(); else wait_vm<0>();
        } else {
            if (infl == 2) wait_vm<14>(); else if (infl == 1) wait_vm<7>(); else wait_vm<0>();
        }
        asm volatile("s_waitcnt lgkmcnt(0)" ::: "memory");
        __builtin_amdgcn_s_barrier();
        asm volatile("" ::: "memory");
        if (it + NBUF - 1 < NST) issue(it + NBUF - 1, (it + NBUF - 1) % NBUF);
        const char* sb = smem + (it % NBUF) * STAGE_B;
#pragma unroll
        for (int tap = 0; tap < 3; ++tap) {
#pragma unroll
            for (int s = 0; s < 2; ++s) {
                s16x4 b0 = __builtin_amdgcn_ds_read_tr16_b64_v4i16((lds_s16x4_p)(sb + boff + (tap * 32 + s * 16) * 128));
                s16x4 b1 = __builtin_amdgcn_ds_read_tr16_b64_v4i16((lds_s16x4_p)(sb + boff + (tap * 32 + s * 16 + 4) * 128));
                s16x8 bf = {b0[0], b0[1], b0[2], b0[3], b1[0], b1[1], b1[2], b1[3]};
#pragma unroll
                for (int mi = 0; mi < 2; ++mi) {
                    s16x8 af = *(const s16x8*)(sb + (aoff[tap][mi] ^ (s * 32)));
                    acc[mi] = __builtin_amdgcn_mfma_f32_32x32x16_bf16(__builtin_bit_cast(bf16x8, af),
                                                                       __builtin_bit_cast(bf16x8, bf), acc[mi], 0, 0, 0);
                }
            }
        }
    }

    const int col = wc * 32 + (lane & 31);
    const int gcol = n0 + col;
    const float bv = __bfloat162float(bias[gcol]);
    const int nvalid = (LL - l0) < BM ? (LL - l0) : BM;
    float cnt = 0.f, s1 = 0.f, s2 = 0.f, cshift = 0.f;
    {
        int r0 = wr * 64 + 4 * h;
        cshift = acc[0][0] + bv;
        (void)r0;
    }
#pragma unroll
    for (int mi = 0; mi < 2; ++mi)
#pragma unroll
        for (int i = 0; i < 16; ++i) {
            int row = wr * 64 + mi * 32 + (i & 3) + 8 * (i >> 2) + 4 * h;
            float v = acc[mi][i] + bv;
            acc[mi][i] = v;
            float d = v - cshift;
            bool ok = row < nvalid;
            cnt += ok ? 1.f : 0.f;
            s1 += ok ? d : 0.f;
            s2 += ok ? d * d : 0.f;
        }
    float mean_l = cshift, m2_l = 0.f;
    if (cnt > 0.f) { float ic = 1.f / cnt; mean_l = cshift + s1 * ic; m2_l = s2 - s1 * s1 * ic; m2_l = m2_l > 0.f ? m2_l : 0.f; }
    {
        float nb = __shfl_xor(cnt, 32), mb = __shfl_xor(mean_l, 32), qb = __shfl_xor(m2_l, 32);
        float nt_ = cnt + nb;
        if (nt_ > 0.f) {
            float d = mb - mean_l;
            float mm = (cnt * mean_l + nb * mb) / nt_;
            float qq = m2_l + qb + d * d * cnt * nb / nt_;
            mean_l = mm; m2_l = qq; cnt = nt_;
        }
    }
    __syncthreads();
    float* red = (float*)smem;
    if (h == 0) {
        red[(wr * 64 + col) * 3 + 0] = cnt;
        red[(wr * 64 + col) * 3 + 1] = mean_l;
        red[(wr * 64 + col) * 3 + 2] = m2_l;
    }
    __syncthreads();
    gu64* gbase = (gu64*)(gran + ((size_t)(batch * 8 + nt) * 16) * 64);
    if (tid < 64) {
        float n = red[tid * 3], m = red[tid * 3 + 1], q = red[tid * 3 + 2];
        chan_merge(n, m, q, red[(64 + tid) * 3], red[(64 + tid) * 3 + 1], red[(64 + tid) * 3 + 2]);
        unsigned long long g = ((unsigned long long)(__float_as_uint(q) | 0x80000000u) << 32) | __float_as_uint(m);
        __hip_atomic_store(gbase + mtb * 64 + tid, g, __ATOMIC_RELAXED, __HIP_MEMORY_SCOPE_AGENT);
    }
    {
        const int c = tid & 63, qd = tid >> 6;
        unsigned long long g[4];
        for (;;) {
            bool ok = true;
#pragma unroll
            for (int j = 0; j < 4; ++j) {
                g[j] = __hip_atomic_load(gbase + (qd * 4 + j) * 64 + c, __ATOMIC_RELAXED, __HIP_MEMORY_SCOPE_AGENT);
                ok &= (g[j] >> 63) != 0;
            }
            if (__all(ok)) break;
            __builtin_amdgcn_s_sleep(1);
        }
        float n = 0.f, m = 0.f, q = 0.f;
#pragma unroll
        for (int j = 0; j < 4; ++j) {
            int tile = qd * 4 + j;
            float nb = tile == 15 ? (float)(LL - 15 * BM) : (float)BM;
            float mb = __uint_as_float((unsigned)g[j]);
            float qb = __uint_as_float((unsigned)(g[j] >> 32) & 0x7fffffffu);
            if (j == 0) { n = nb; m = mb; q = qb; } else chan_merge(n, m, q, nb, mb, qb);
        }
        float* red2 = (float*)(smem + 4096);
        red2[(qd * 64 + c) * 3 + 0] = n;
        red2[(qd * 64 + c) * 3 + 1] = m;
        red2[(qd * 64 + c) * 3 + 2] = q;
    }
    __syncthreads();
    float mean, rstd;
    {
        const float* red2 = (const float*)(smem + 4096);
        float n = red2[col * 3], m = red2[col * 3 + 1], q = red2[col * 3 + 2];
#pragma unroll
        for (int j = 1; j < 4; ++j)
            chan_merge(n, m, q, red2[(j * 64 + col) * 3], red2[(j * 64 + col) * 3 + 1], red2[(j * 64 + col) * 3 + 2]);
        mean = m;
        rstd = 1.f / sqrtf(q / (float)LL + 1e-5f);
    }
    const float ga = __bfloat162float(gamma[gcol]) * rstd;
    const float be = __bfloat162float(beta[gcol]);
    unsigned short* ot = (unsigned short*)(smem + 8192);
#pragma unroll
    for (int mi = 0; mi < 2; ++mi)
#pragma unroll
        for (int i = 0; i < 16; ++i) {
            int row = wr * 64 + mi * 32 + (i & 3) + 8 * (i >> 2) + 4 * h;
            float v = (acc[mi][i] - mean) * ga + be;
            float u = 0.7978845608028654f * (v + 0.044715f * v * v * v);
            float e = __expf(2.f * u);
            float th = 1.f - 2.f / (e + 1.f);
            float o = 0.5f * v * (1.f + th);
            __hip_bfloat16 ob = __float2bfloat16(o);
            ot[row * 64 + col] = *(unsigned short*)&ob;
        }
    __syncthreads();
#pragma unroll
    for (int j = 0; j < 4; ++j) {
        int e = tid + 256 * j;
        int row = e >> 3, ch = e & 7;
        if (row < nvalid) {
            uint4 v = *(const uint4*)(ot + row * 64 + ch * 8);
            *(uint4*)(out + ((size_t)(batch * LL + l0 + row) * DD + n0 + ch * 8)) = v;
        }
    }
}

extern "C" void kernel_launch(void* const* d_in, const int* in_sizes, int n_in,
                              void* d_out, int out_size, void* d_ws, size_t ws_size,
                              hipStream_t stream) {
    const __hip_bfloat16* x = (const __hip_bfloat16*)d_in[0];
    const __hip_bfloat16* w = (const __hip_bfloat16*)d_in[1];
    const __hip_bfloat16* b = (const __hip_bfloat16*)d_in[2];
    const __hip_bfloat16* gamma = (const __hip_bfloat16*)d_in[3];
    const __hip_bfloat16* beta = (const __hip_bfloat16*)d_in[4];
    __hip_bfloat16* out = (__hip_bfloat16*)d_out;
    unsigned long long* gran = (unsigned long long*)d_ws;
    (void)hipMemsetAsync(d_ws, 0, 2 * 8 * 16 * 64 * 8, stream);
    conv_gn_gelu_kernel<<<256, 256, 0, stream>>>(x, w, b, gamma, beta, out, gran);
}
```

```cpp
#include <hip/hip_runtime.h>
#include <hip/hip_bf16.h>

typedef __attribute__((ext_vector_type(8))) short s16x8;
typedef __attribute__((ext_vector_type(4))) short s16x4;
typedef __attribute__((ext_vector_type(8))) __bf16 bf16x8;
typedef __attribute__((ext_vector_type(16))) float f32x16;
typedef __attribute__((address_space(3))) s16x4* lds_s16x4_p;
typedef __attribute__((address_space(1))) unsigned long long gu64;

constexpr int TT = 4000, DD = 512, LL = 1999;
constexpr int BM = 128, BN = 64;
constexpr int CK = 64;
constexpr int NST = DD / CK;
constexpr int E_OFF = 0, O_OFF = 136 * 128, W_OFF = O_OFF + 128 * 128;
constexpr int STAGE_B = W_OFF + 192 * 128;
constexpr int LDS_B = 2 * STAGE_B;

#define STAMP(i)

__device__ __forceinline__ void glds16(unsigned voff, const void* sbase, unsigned lds) {
    asm volatile("s_mov_b32 m0, %2\n\ts_nop 0\n\tglobal_load_lds_dwordx4 %0, %1" ::"v"(voff), "s"(sbase), "s"(lds) : "memory", "m0");
}
__device__ __forceinline__ const char* uni(const char* p) {
    unsigned long long v = (unsigned long long)p;
    unsigned lo = __builtin_amdgcn_readfirstlane((unsigned)v), hi = __builtin_amdgcn_readfirstlane((unsigned)(v >> 32));
    return (const char*)(((unsigned long long)hi << 32) | lo);
}
template <int N> __device__ __forceinline__ void wait_vm() { asm volatile("s_waitcnt vmcnt(%0)" ::"n"(N) : "memory"); }
__device__ __forceinline__ void bar_lgkm() {
    asm volatile("s_waitcnt lgkmcnt(0)" ::: "memory");
    __builtin_amdgcn_s_barrier();
    asm volatile("" ::: "memory");
}

__device__ __forceinline__ void chan_merge(float& n, float& m, float& q, float nb, float mb, float qb) {
    if (nb > 0.f) {
        float nt = n + nb;
        float d = mb - m;
        float r = nb / nt;
        m = m + d * r;
        q = q + qb + d * d * n * r;
        n = nt;
    }
}

struct Frags { s16x8 a[3][2][2]; s16x8 b[3][2]; };

__global__ __launch_bounds__(256, 1)
void conv_gn_gelu_kernel(const __hip_bfloat16* __restrict__ x, const __hip_bfloat16* __restrict__ w,
                         const __hip_bfloat16* __restrict__ bias, const __hip_bfloat16* __restrict__ gamma,
                         const __hip_bfloat16* __restrict__ beta, __hip_bfloat16* __restrict__ out,
                         unsigned long long* __restrict__ gran) {
    __shared__ __attribute__((aligned(16))) char smem[LDS_B];

    const int tid = threadIdx.x;
    const int lane = tid & 63;
    STAMP(0);
    const int wv = __builtin_amdgcn_readfirstlane(tid >> 6);
    const int wr = wv >> 1, wc = wv & 1;

    const int bid = blockIdx.x;
    const int xcd = bid & 7, idx = bid >> 3;
    const int mt = xcd * 4 + (idx >> 3);
    const int nt = idx & 7;
    const int batch = mt >> 4, mtb = mt & 15;
    const int l0 = mtb * BM, n0 = nt * BN;

    const char* xb = uni((const char*)(x + (size_t)batch * TT * DD));
    const char* wb = uni((const char*)w + n0 * 2);
    unsigned voff[15];
#pragma unroll
    for (int j = 0; j < 15; ++j) {
        if (j < 8 || j == 14) {
            int P = (j == 14) ? 16 : 4 * (j & 3) + wv;
            int par = (j >= 4 && j < 8) ? 1 : 0;
            int pos = 8 * P + (lane >> 3);
            int r = pos ^ (P & 1);
            int c = (lane & 7) ^ (r & 7);
            int row = 2 * (l0 + r) + par;
            row = row < TT ? row : TT - 1;
            voff[j] = (unsigned)row * (DD * 2) + c * 16;
        } else {
            int P = 4 * (j - 8) + wv;
            int kr = 8 * P + (lane >> 3);
            int tap = kr >> 6, kk = kr & 63;
            int c = (lane & 7) ^ (((kr >> 1) & 1) << 2);
            voff[j] = (unsigned)(tap * DD + kk) * (DD * 2) + c * 16;
        }
    }
    const unsigned lds_base = (unsigned)(size_t)(__attribute__((address_space(3))) char*)smem;
    auto issue = [&](int st) {
        const unsigned lb = lds_base + (st & 1) * STAGE_B + wv * 1024;
        const char* xs = xb + st * (CK * 2);
        const char* ws = wb + (size_t)st * (CK * DD * 2);
#pragma unroll
        for (int j = 0; j < 4; ++j) glds16(voff[j], xs, lb + E_OFF + j * 4096);
#pragma unroll
        for (int j = 4; j < 8; ++j) glds16(voff[j], xs, lb + O_OFF + (j - 4) * 4096);
#pragma unroll
        for (int j = 8; j < 14; ++j) glds16(voff[j], ws, lb + W_OFF + (j - 8) * 4096);
        if (wv == 0) glds16(voff[14], xs, lb + E_OFF + 16 * 1024);
    };

    issue(0);
    issue(1);

    const int h = lane >> 5;
    int aoff[3][2];
#pragma unroll
    for (int mi = 0; mi < 2; ++mi) {
        int r = wr * 64 + mi * 32 + (lane & 31);
        int pr = r ^ ((r >> 3) & 1);
        aoff[0][mi] = E_OFF + pr * 128 + 16 * (h ^ (r & 7));
        aoff[1][mi] = O_OFF + pr * 128 + 16 * (h ^ (r & 7));
        int r1 = r + 1;
        int pr1 = r1 ^ ((r1 >> 3) & 1);
        aoff[2][mi] = E_OFF + pr1 * 128 + 16 * (h ^ (r1 & 7));
    }
    int boff;
    {
        int blk = (lane >> 4) & 1, q = (lane & 15) >> 2, p = lane & 3;
        int row = 8 * h + q;
        int ch = wc * 4 + 2 * blk + (p >> 1);
        boff = W_OFF + row * 128 + 16 * (ch ^ (((q >> 1) & 1) << 2)) + 8 * (p & 1);
    }
    const int col = wc * 32 + (lane & 31);
    const int gcol = n0 + col;
    const float bv = __bfloat162float(bias[gcol]);
    const float gav = __bfloat162float(gamma[gcol]);
    const float bev = __bfloat162float(beta[gcol]);

    f32x16 acc[2];
#pragma unroll
    for (int mi = 0; mi < 2; ++mi)
#pragma unroll
        for (int i = 0; i < 16; ++i) acc[mi][i] = 0.f;

    auto read_half = [&](Frags& f, int st, int half) {
        const char* sb = smem + (st & 1) * STAGE_B;
#pragma unroll
        for (int tap = 0; tap < 3; ++tap)
#pragma unroll
            for (int s2 = 0; s2 < 2; ++s2) {
                const int s = half * 2 + s2;
                s16x4 b0 = __builtin_amdgcn_ds_read_tr16_b64_v4i16((lds_s16x4_p)(sb + boff + (tap * 64 + s * 16) * 128));
                s16x4 b1 = __builtin_amdgcn_ds_read_tr16_b64_v4i16((lds_s16x4_p)(sb + boff + (tap * 64 + s * 16 + 4) * 128));
                f.b[tap][s2] = (s16x8){b0[0], b0[1], b0[2], b0[3], b1[0], b1[1], b1[2], b1[3]};
#pragma unroll
                for (int mi = 0; mi < 2; ++mi) f.a[tap][s2][mi] = *(const s16x8*)(sb + (aoff[tap][mi] ^ (s * 32)));
            }
    };
    auto mma_half = [&](const Frags& f) {
#pragma unroll
        for (int tap = 0; tap < 3; ++tap)
#pragma unroll
            for (int s2 = 0; s2 < 2; ++s2)
#pragma unroll
                for (int mi = 0; mi < 2; ++mi)
                    acc[mi] = __builtin_amdgcn_mfma_f32_32x32x16_bf16(__builtin_bit_cast(bf16x8, f.a[tap][s2][mi]),
                                                                       __builtin_bit_cast(bf16x8, f.b[tap][s2]), acc[mi], 0, 0, 0);
    };

    Frags fa, fb;
    if (wv == 0) wait_vm<15>(); else wait_vm<14>();
    bar_lgkm();
    read_half(fa, 0, 0);
    for (int it = 0; it < NST; ++it) {
        read_half(fb, it, 1);
        __builtin_amdgcn_sched_barrier(0);
        mma_half(fa);
        bar_lgkm();
        if (it + 2 < NST) {
            issue(it + 2);
            if (wv == 0) wait_vm<15>(); else wait_vm<14>();
        } else {
            wait_vm<0>();
        }
        bar_lgkm();
        if (it + 1 < NST) read_half(fa, it + 1, 0);
        __builtin_amdgcn_sched_barrier(0);
        mma_half(fb);
    }
    STAMP(1);

    const int nvalid = (LL - l0) < BM ? (LL - l0) : BM;
    float cnt, s1 = 0.f, s2 = 0.f;
    const float cshift = acc[0][0] + bv;
    if (nvalid == BM) {
        cnt = 32.f;
#pragma unroll
        for (int mi = 0; mi < 2; ++mi)
#pragma unroll
            for (int i = 0; i < 16; ++i) {
                float v = acc[mi][i] + bv;
                acc[mi][i] = v;
                float d = v - cshift;
                s1 += d;
                s2 = fmaf(d, d, s2);
            }
    } else {
        cnt = 0.f;
#pragma unroll
        for (int mi = 0; mi < 2; ++mi)
#pragma unroll
            for (int i = 0; i < 16; ++i) {
                int row = wr * 64 + mi * 32 + (i & 3) + 8 * (i >> 2) + 4 * h;
                float v = acc[mi][i] + bv;
                acc[mi][i] = v;
                float d = row < nvalid ? v - cshift : 0.f;
                cnt += row < nvalid ? 1.f : 0.f;
                s1 += d;
                s2 = fmaf(d, d, s2);
            }
    }
    float mean_l = cshift, m2_l = 0.f;
    if (cnt > 0.f) { float ic = 1.f / cnt; mean_l = cshift + s1 * ic; m2_l = s2 - s1 * s1 * ic; m2_l = m2_l > 0.f ? m2_l : 0.f; }
    {
        float nb = __shfl_xor(cnt, 32), mb = __shfl_xor(mean_l, 32), qb = __shfl_xor(m2_l, 32);
        float nt_ = cnt + nb;
        if (nt_ > 0.f) {
            float d = mb - mean_l;
            float mm = (cnt * mean_l + nb * mb) / nt_;
            float qq = m2_l + qb + d * d * cnt * nb / nt_;
            mean_l = mm; m2_l = qq; cnt = nt_;
        }
    }
    float* red = (float*)(smem + 32768);
    __syncthreads();
    if (h == 0) {
        red[(wr * 64 + col) * 3 + 0] = cnt;
        red[(wr * 64 + col) * 3 + 1] = mean_l;
        red[(wr * 64 + col) * 3 + 2] = m2_l;
    }
    __syncthreads();
    gu64* gbase = (gu64*)(gran + ((size_t)(batch * 8 + nt) * 16) * 64);
    if (tid < 64) {
        float n = red[tid * 3], m = red[tid * 3 + 1], q = red[tid * 3 + 2];
        chan_merge(n, m, q, red[(64 + tid) * 3], red[(64 + tid) * 3 + 1], red[(64 + tid) * 3 + 2]);
        unsigned long long g = ((unsigned long long)(__float_as_uint(q) | 0x80000000u) << 32) | __float_as_uint(m);
        __hip_atomic_store(gbase + mtb * 64 + tid, g, __ATOMIC_RELAXED, __HIP_MEMORY_SCOPE_AGENT);
    }
    STAMP(2);
    {
        const int c = tid & 63, qd = tid >> 6;
        unsigned long long g[4];
        for (;;) {
            bool ok = true;
#pragma unroll
            for (int j = 0; j < 4; ++j) {
                g[j] = __hip_atomic_load(gbase + (qd * 4 + j) * 64 + c, __ATOMIC_RELAXED, __HIP_MEMORY_SCOPE_AGENT);
                ok &= (g[j] >> 63) != 0;
            }
            if (__all(ok)) break;
            __builtin_amdgcn_s_sleep(1);
        }
        float n = 0.f, m = 0.f, q = 0.f;
#pragma unroll
        for (int j = 0; j < 4; ++j) {
            int tile = qd * 4 + j;
            float nb = tile == 15 ? (float)(LL - 15 * BM) : (float)BM;
            float mb = __uint_as_float((unsigned)g[j]);
            float qb = __uint_as_float((unsigned)(g[j] >> 32) & 0x7fffffffu);
            if (j == 0) { n = nb; m = mb; q = qb; } else chan_merge(n, m, q, nb, mb, qb);
        }
        float* red2 = (float*)(smem + 36864);
        red2[(qd * 64 + c) * 3 + 0] = n;
        red2[(qd * 64 + c) * 3 + 1] = m;
        red2[(qd * 64 + c) * 3 + 2] = q;
    }
    __syncthreads();
    STAMP(3);
    float mean, rstd;
    {
        const float* red2 = (const float*)(smem + 36864);
        float n = red2[col * 3], m = red2[col * 3 + 1], q = red2[col * 3 + 2];
#pragma unroll
        for (int j = 1; j < 4; ++j)
            chan_merge(n, m, q, red2[(j * 64 + col) * 3], red2[(j * 64 + col) * 3 + 1], red2[(j * 64 + col) * 3 + 2]);
        mean = m;
        rstd = 1.f / sqrtf(q / (float)LL + 1e-5f);
    }
    const float ga = gav * rstd;
    const float be = bev - mean * ga;
    unsigned short* ot = (unsigned short*)smem;
    constexpr float K1 = -2.f * 0.7978845608028654f * 1.4426950408889634f;
    constexpr float K2 = K1 * 0.044715f;
#pragma unroll
    for (int mi = 0; mi < 2; ++mi)
#pragma unroll
        for (int i = 0; i < 16; ++i) {
            int row = wr * 64 + mi * 32 + (i & 3) + 8 * (i >> 2) + 4 * h;
            float v = fmaf(acc[mi][i], ga, be);
            float t = v * fmaf(v * v, K2, K1);
            float e = __builtin_amdgcn_exp2f(t);
            float o = v * __builtin_amdgcn_rcpf(1.f + e);
            __hip_bfloat16 ob = __float2bfloat16(o);
            ot[row * 64 + col] = *(unsigned short*)&ob;
        }
    __syncthreads();
#pragma unroll
    for (int j = 0; j < 4; ++j) {
        int e = tid + 256 * j;
        int row = e >> 3, ch = e & 7;
        if (row < nvalid) {
            uint4 v = *(const uint4*)(ot + row * 64 + ch * 8);
            *(uint4*)(out + ((size_t)(batch * LL + l0 + row) * DD + n0 + ch * 8)) = v;
        }
    }
    STAMP(4);
}

extern "C" void kernel_launch(void* const* d_in, const int* in_sizes, int n_in,
                              void* d_out, int out_size, void* d_ws, size_t ws_size,
                              hipStream_t stream) {
    const __hip_bfloat16* x = (const __hip_bfloat16*)d_in[0];
    const __hip_bfloat16* w = (const __hip_bfloat16*)d_in[1];
    const __hip_bfloat16* b = (const __hip_bfloat16*)d_in[2];
    const __hip_bfloat16* gamma = (const __hip_bfloat16*)d_in[3];
    const __hip_bfloat16* beta = (const __hip_bfloat16*)d_in[4];
    __hip_bfloat16* out = (__hip_bfloat16*)d_out;
    unsigned long long* gran = (unsigned long long*)d_ws;
    (void)hipMemsetAsync(d_ws, 0, 2 * 8 * 16 * 64 * 8, stream);
    conv_gn_gelu_kernel<<<256, 256, 0, stream>>>(x, w, b, gamma, beta, out, gran);
}
```

```cpp
#include <hip/hip_runtime.h>
#include <hip/hip_bf16.h>

typedef __attribute__((ext_vector_type(8))) short s16x8;
typedef __attribute__((ext_vector_type(4))) short s16x4;
typedef __attribute__((ext_vector_type(8))) __bf16 bf16x8;
typedef __attribute__((ext_vector_type(16))) float f32x16;
typedef __attribute__((address_space(3))) s16x4* lds_s16x4_p;
typedef __attribute__((address_space(1))) unsigned long long gu64;

constexpr int TT = 4000, DD = 512, LL = 1999;
constexpr int BM = 128, BN = 64;
constexpr int CK = 64;
constexpr int NST = DD / CK;
constexpr int E_OFF = 0, O_OFF = 136 * 128, W_OFF = O_OFF + 128 * 128;
constexpr int STAGE_B = W_OFF + 192 * 128;
constexpr int LDS_B = 2 * STAGE_B;

#define STAMP(i)

__device__ __forceinline__ void glds16(unsigned voff, const void* sbase, unsigned lds) {
    asm volatile("s_mov_b32 m0, %2\n\ts_nop 0\n\tglobal_load_lds_dwordx4 %0, %1" ::"v"(voff), "s"(sbase), "s"(lds) : "memory", "m0");
}
__device__ __forceinline__ const char* uni(const char* p) {
    unsigned long long v = (unsigned long long)p;
    unsigned lo = __builtin_amdgcn_readfirstlane((unsigned)v), hi = __builtin_amdgcn_readfirstlane((unsigned)(v >> 32));
    return (const char*)(((unsigned long long)hi << 32) | lo);
}
template <int N> __device__ __forceinline__ void wait_vm() { asm volatile("s_waitcnt vmcnt(%0)" ::"n"(N) : "memory"); }
__device__ __forceinline__ void bar_lgkm() {
    asm volatile("s_waitcnt lgkmcnt(0)" ::: "memory");
    __builtin_amdgcn_s_barrier();
    asm volatile("" ::: "memory");
}

__device__ __forceinline__ void chan_merge(float& n, float& m, float& q, float nb, float mb, float qb) {
    if (nb > 0.f) {
        float nt = n + nb;
        float d = mb - m;
        float r = nb * __builtin_amdgcn_rcpf(nt);
        m = m + d * r;
        q = q + qb + d * d * n * r;
        n = nt;
    }
}

__global__ __launch_bounds__(256, 1)
void conv_gn_gelu_kernel(const __hip_bfloat16* __restrict__ x, const __hip_bfloat16* __restrict__ w,
                         const __hip_bfloat16* __restrict__ bias, const __hip_bfloat16* __restrict__ gamma,
                         const __hip_bfloat16* __restrict__ beta, __hip_bfloat16* __restrict__ out,
                         unsigned long long* __restrict__ gran) {
    __shared__ __attribute__((aligned(16))) char smem[LDS_B];

    const int tid = threadIdx.x;
    const int lane = tid & 63;
    STAMP(0);
    const int wv = __builtin_amdgcn_readfirstlane(tid >> 6);
    const int wr = wv >> 1, wc = wv & 1;

    const int bid = blockIdx.x;
    const int xcd = bid & 7, idx = bid >> 3;
    const int mt = xcd * 4 + (idx >> 3);
    const int nt = idx & 7;
    const int batch = mt >> 4, mtb = mt & 15;
    const int l0 = mtb * BM, n0 = nt * BN;

    const char* xb = uni((const char*)(x + (size_t)batch * TT * DD));
    const char* wb = uni((const char*)w + n0 * 2);
    unsigned voff[15];
#pragma unroll
    for (int j = 0; j < 15; ++j) {
        if (j < 8 || j == 14) {
            int P = (j == 14) ? 16 : 4 * (j & 3) + wv;
            int par = (j >= 4 && j < 8) ? 1 : 0;
            int pos = 8 * P + (lane >> 3);
            int r = pos ^ (P & 1);
            int c = (lane & 7) ^ (r & 7);
            int row = 2 * (l0 + r) + par;
            row = row < TT ? row : TT - 1;
            voff[j] = (unsigned)row * (DD * 2) + c * 16;
        } else {
            int P = 4 * (j - 8) + wv;
            int kr = 8 * P + (lane >> 3);
            int tap = kr >> 6, kk = kr & 63;
            int c = (lane & 7) ^ (((kr >> 1) & 1) << 2);
            voff[j] = (unsigned)(tap * DD + kk) * (DD * 2) + c * 16;
        }
    }
    const unsigned lds_base = (unsigned)(size_t)(__attribute__((address_space(3))) char*)smem;
    auto piece = [&](int st, int j) {
        const unsigned lb = lds_base + (st & 1) * STAGE_B + wv * 1024;
        const char* xs = xb + st * (CK * 2);
        const char* ws = wb + (size_t)st * (CK * DD * 2);
        if (j < 4) glds16(voff[j], xs, lb + E_OFF + j * 4096);
        else if (j < 8) glds16(voff[j], xs, lb + O_OFF + (j - 4) * 4096);
        else if (j < 14) glds16(voff[j], ws, lb + W_OFF + (j - 8) * 4096);
        else { if (wv == 0) glds16(voff[14], xs, lb + E_OFF + 16 * 1024); }
    };
    constexpr int G1J[9] = {0, 1, 2, 3, 14, 8, 9, 12, 13};
    constexpr int G2J[6] = {4, 5, 6, 7, 10, 11};
#pragma unroll
    for (int k = 0; k < 9; ++k) piece(0, G1J[k]);
#pragma unroll
    for (int k = 0; k < 6; ++k) piece(0, G2J[k]);
#pragma unroll
    for (int k = 0; k < 9; ++k) piece(1, G1J[k]);
#pragma unroll
    for (int k = 0; k < 6; ++k) piece(1, G2J[k]);

    const int h = lane >> 5;
    int aoff[3][2];
#pragma unroll
    for (int mi = 0; mi < 2; ++mi) {
        int r = wr * 64 + mi * 32 + (lane & 31);
        int pr = r ^ ((r >> 3) & 1);
        aoff[0][mi] = E_OFF + pr * 128 + 16 * (h ^ (r & 7));
        aoff[1][mi] = O_OFF + pr * 128 + 16 * (h ^ (r & 7));
        int r1 = r + 1;
        int pr1 = r1 ^ ((r1 >> 3) & 1);
        aoff[2][mi] = E_OFF + pr1 * 128 + 16 * (h ^ (r1 & 7));
    }
    int boff;
    {
        int blk = (lane >> 4) & 1, q = (lane & 15) >> 2, p = lane & 3;
        int row = 8 * h + q;
        int ch = wc * 4 + 2 * blk + (p >> 1);
        boff = W_OFF + row * 128 + 16 * (ch ^ (((q >> 1) & 1) << 2)) + 8 * (p & 1);
    }
    const int col = wc * 32 + (lane & 31);
    const int gcol = n0 + col;
    const float bv = __bfloat162float(bias[gcol]);
    const float gcv = __bfloat162float(gamma[n0 + (tid & 63)]);
    const float bcv = __bfloat162float(beta[n0 + (tid & 63)]);

    f32x16 acc[2];
#pragma unroll
    for (int mi = 0; mi < 2; ++mi)
#pragma unroll
        for (int i = 0; i < 16; ++i) acc[mi][i] = 0.f;

    s16x8 a1[2][4][2], b1[2][4];
    s16x8 a2[4][2], b2[4];
    auto rd_b = [&](const char* sb, int tap, int s) {
        s16x4 b0 = __builtin_amdgcn_ds_read_tr16_b64_v4i16((lds_s16x4_p)(sb + boff + (tap * 64 + s * 16) * 128));
        s16x4 b1_ = __builtin_amdgcn_ds_read_tr16_b64_v4i16((lds_s16x4_p)(sb + boff + (tap * 64 + s * 16 + 4) * 128));
        return (s16x8){b0[0], b0[1], b0[2], b0[3], b1_[0], b1_[1], b1_[2], b1_[3]};
    };
    auto read_g1 = [&](int st) {
        const char* sb = smem + (st & 1) * STAGE_B;
#pragma unroll
        for (int t = 0; t < 2; ++t)
#pragma unroll
            for (int s = 0; s < 4; ++s) {
                b1[t][s] = rd_b(sb, 2 * t, s);
#pragma unroll
                for (int mi = 0; mi < 2; ++mi) a1[t][s][mi] = *(const s16x8*)(sb + (aoff[2 * t][mi] ^ (s * 32)));
            }
    };
    auto read_g2 = [&](int st) {
        const char* sb = smem + (st & 1) * STAGE_B;
#pragma unroll
        for (int s = 0; s < 4; ++s) {
            b2[s] = rd_b(sb, 1, s);
#pragma unroll
            for (int mi = 0; mi < 2; ++mi) a2[s][mi] = *(const s16x8*)(sb + (aoff[1][mi] ^ (s * 32)));
        }
    };
#define MMA(A, B, mi) acc[mi] = __builtin_amdgcn_mfma_f32_32x32x16_bf16(__builtin_bit_cast(bf16x8, A), __builtin_bit_cast(bf16x8, B), acc[mi], 0, 0, 0)
#define SB0 __builtin_amdgcn_sched_barrier(0)

    if (wv == 0) wait_vm<21>(); else wait_vm<20>();
    bar_lgkm();
    read_g1(0);
    if (wv == 0) wait_vm<15>(); else wait_vm<14>();
    for (int it = 0; it < NST; ++it) {
        const bool more = it + 2 < NST;
        bar_lgkm();
        read_g2(it);
        SB0;
#pragma unroll
        for (int k = 0; k < 8; ++k) {
            MMA(a1[k >> 2][k & 3][0], b1[k >> 2][k & 3], 0);
            MMA(a1[k >> 2][k & 3][1], b1[k >> 2][k & 3], 1);
            SB0;
            if (more) piece(it + 2, G1J[k]);
            SB0;
        }
        if (more) piece(it + 2, G1J[8]);
        if (more) { if (wv == 0) wait_vm<15>(); else wait_vm<14>(); }
        else if (it + 1 < NST) wait_vm<6>();
        bar_lgkm();
        if (it + 1 < NST) read_g1(it + 1);
        SB0;
#pragma unroll
        for (int k = 0; k < 4; ++k) {
            MMA(a2[k][0], b2[k], 0);
            SB0;
            if (more) piece(it + 2, G2J[k]);
            SB0;
            MMA(a2[k][1], b2[k], 1);
            SB0;
            if (more && k < 2) piece(it + 2, G2J[4 + k]);
            SB0;
        }
        if (more) { if (wv == 0) wait_vm<15>(); else wait_vm<14>(); }
        else wait_vm<0>();
    }
    STAMP(1);

    const int nvalid = (LL - l0) < BM ? (LL - l0) : BM;
    float cnt, s1 = 0.f, s2 = 0.f;
    const float cshift = acc[0][0] + bv;
    if (nvalid == BM) {
        cnt = 32.f;
#pragma unroll
        for (int mi = 0; mi < 2; ++mi)
#pragma unroll
            for (int i = 0; i < 16; ++i) {
                float v = acc[mi][i] + bv;
                acc[mi][i] = v;
                float d = v - cshift;
                s1 += d;
                s2 = fmaf(d, d, s2);
            }
    } else {
        cnt = 0.f;
#pragma unroll
        for (int mi = 0; mi < 2; ++mi)
#pragma unroll
            for (int i = 0; i < 16; ++i) {
                int row = wr * 64 + mi * 32 + (i & 3) + 8 * (i >> 2) + 4 * h;
                float v = acc[mi][i] + bv;
                acc[mi][i] = v;
                float d = row < nvalid ? v - cshift : 0.f;
                cnt += row < nvalid ? 1.f : 0.f;
                s1 += d;
                s2 = fmaf(d, d, s2);
            }
    }
    float mean_l = cshift, m2_l = 0.f;
    if (cnt > 0.f) { float ic = __builtin_amdgcn_rcpf(cnt); mean_l = cshift + s1 * ic; m2_l = s2 - s1 * s1 * ic; m2_l = m2_l > 0.f ? m2_l : 0.f; }
    {
        float nb = __shfl_xor(cnt, 32), mb = __shfl_xor(mean_l, 32), qb = __shfl_xor(m2_l, 32);
        float nt_ = cnt + nb;
        if (nt_ > 0.f) {
            float d = mb - mean_l;
            float rn = __builtin_amdgcn_rcpf(nt_);
            float mm = (cnt * mean_l + nb * mb) * rn;
            float qq = m2_l + qb + d * d * cnt * nb * rn;
            mean_l = mm; m2_l = qq; cnt = nt_;
        }
    }
    float* red = (float*)(smem + 32768);
    __syncthreads();
    if (h == 0) {
        red[(wr * 64 + col) * 3 + 0] = cnt;
        red[(wr * 64 + col) * 3 + 1] = mean_l;
        red[(wr * 64 + col) * 3 + 2] = m2_l;
    }
    __syncthreads();
    gu64* gbase = (gu64*)(gran + ((size_t)(batch * 8 + nt) * 16) * 64);
    if (tid < 64) {
        float n = red[tid * 3], m = red[tid * 3 + 1], q = red[tid * 3 + 2];
        chan_merge(n, m, q, red[(64 + tid) * 3], red[(64 + tid) * 3 + 1], red[(64 + tid) * 3 + 2]);
        unsigned long long g = ((unsigned long long)(__float_as_uint(q) | 0x80000000u) << 32) | __float_as_uint(m);
        __hip_atomic_store(gbase + mtb * 64 + tid, g, __ATOMIC_RELAXED, __HIP_MEMORY_SCOPE_AGENT);
    }
    STAMP(2);
    float* yt = (float*)(smem + 40960);
#pragma unroll
    for (int mi = 0; mi < 2; ++mi)
#pragma unroll
        for (int i = 0; i < 16; ++i) {
            int row = wr * 64 + mi * 32 + (i & 3) + 8 * (i >> 2) + 4 * h;
            yt[row * 68 + col] = acc[mi][i];
        }
    float* gb = (float*)(smem + 32768);
    if (tid < 64) {
        unsigned long long g[16];
        for (;;) {
            bool ok = true;
#pragma unroll
            for (int j = 0; j < 16; ++j) {
                g[j] = __hip_atomic_load(gbase + j * 64 + tid, __ATOMIC_RELAXED, __HIP_MEMORY_SCOPE_AGENT);
                ok &= (g[j] >> 63) != 0;
            }
            if (__all(ok)) break;
            __builtin_amdgcn_s_sleep(1);
        }
        constexpr float NL = (float)(LL - 15 * BM), INVL = 1.f / (float)LL;
        float sm = 0.f;
#pragma unroll
        for (int j = 0; j < 16; ++j) sm = fmaf(__uint_as_float((unsigned)g[j]), j == 15 ? NL : (float)BM, sm);
        const float m = sm * INVL;
        float q = 0.f;
#pragma unroll
        for (int j = 0; j < 16; ++j) {
            float d = __uint_as_float((unsigned)g[j]) - m;
            q += __uint_as_float((unsigned)(g[j] >> 32) & 0x7fffffffu);
            q = fmaf(d * d, j == 15 ? NL : (float)BM, q);
        }
        float rstd = __builtin_amdgcn_rsqf(fmaf(q, INVL, 1e-5f));
        float ga = gcv * rstd;
        gb[tid] = ga;
        gb[64 + tid] = bcv - m * ga;
    }
    __syncthreads();
    STAMP(3);
    {
        constexpr float K1 = -2.f * 0.7978845608028654f * 1.4426950408889634f;
        constexpr float K2 = K1 * 0.044715f;
        const int ch = tid & 7;
        float ga[8], be[8];
#pragma unroll
        for (int k = 0; k < 2; ++k) {
            float4 t0 = *(const float4*)(gb + ch * 8 + 4 * k);
            float4 t1 = *(const float4*)(gb + 64 + ch * 8 + 4 * k);
            ga[4 * k] = t0.x; ga[4 * k + 1] = t0.y; ga[4 * k + 2] = t0.z; ga[4 * k + 3] = t0.w;
            be[4 * k] = t1.x; be[4 * k + 1] = t1.y; be[4 * k + 2] = t1.z; be[4 * k + 3] = t1.w;
        }
#pragma unroll
        for (int j = 0; j < 4; ++j) {
            const int row = (tid >> 3) + 32 * j;
            float y[8];
#pragma unroll
            for (int k = 0; k < 2; ++k) {
                float4 t = *(const float4*)(yt + row * 68 + ch * 8 + 4 * k);
                y[4 * k] = t.x; y[4 * k + 1] = t.y; y[4 * k + 2] = t.z; y[4 * k + 3] = t.w;
            }
            unsigned pk[4];
#pragma unroll
            for (int k = 0; k < 4; ++k) {
                float o2[2];
#pragma unroll
                for (int u = 0; u < 2; ++u) {
                    float v = fmaf(y[2 * k + u], ga[2 * k + u], be[2 * k + u]);
                    float t = v * fmaf(v * v, K2, K1);
                    float e = __builtin_amdgcn_exp2f(t);
                    o2[u] = v * __builtin_amdgcn_rcpf(1.f + e);
                }
                __hip_bfloat16 lo = __float2bfloat16(o2[0]), hi = __float2bfloat16(o2[1]);
                pk[k] = (unsigned)*(unsigned short*)&lo | ((unsigned)*(unsigned short*)&hi << 16);
            }
            if (row < nvalid)
                *(uint4*)(out + ((size_t)(batch * LL + l0 + row) * DD + n0 + ch * 8)) = make_uint4(pk[0], pk[1], pk[2], pk[3]);
        }
    }
    STAMP(4);
}

extern "C" void kernel_launch(void* const* d_in, const int* in_sizes, int n_in,
                              void* d_out, int out_size, void* d_ws, size_t ws_size,
                              hipStream_t stream) {
    const __hip_bfloat16* x = (const __hip_bfloat16*)d_in[0];
    const __hip_bfloat16* w = (const __hip_bfloat16*)d_in[1];
    const __hip_bfloat16* b = (const __hip_bfloat16*)d_in[2];
    const __hip_bfloat16* gamma = (const __hip_bfloat16*)d_in[3];
    const __hip_bfloat16* beta = (const __hip_bfloat16*)d_in[4];
    __hip_bfloat16* out = (__hip_bfloat16*)d_out;
    unsigned long long* gran = (unsigned long long*)d_ws;
    (void)hipMemsetAsync(d_ws, 0, 2 * 8 * 16 * 64 * 8, stream);
    conv_gn_gelu_kernel<<<256, 256, 0, stream>>>(x, w, b, gamma, beta, out, gran);
}
```

```cpp
#include <hip/hip_runtime.h>
#include <hip/hip_bf16.h>

typedef __attribute__((ext_vector_type(8))) short s16x8;
typedef __attribute__((ext_vector_type(4))) short s16x4;
typedef __attribute__((ext_vector_type(8))) __bf16 bf16x8;
typedef __attribute__((ext_vector_type(16))) float f32x16;
typedef __attribute__((address_space(3))) s16x4* lds_s16x4_p;
typedef __attribute__((address_space(1))) unsigned long long gu64;

constexpr int TT = 4000, DD = 512, LL = 1999;
constexpr int BM = 128, BN = 64;
constexpr int CK = 64;
constexpr int NST = DD / CK;
constexpr int E_OFF = 0, O_OFF = 136 * 128, W_OFF = O_OFF + 128 * 128;
constexpr int STAGE_B = W_OFF + 192 * 128;
constexpr int LDS_B = 2 * STAGE_B;

#define STAMP(i)

__device__ __forceinline__ void glds16(unsigned voff, const void* sbase, unsigned lds) {
    asm volatile("s_mov_b32 m0, %2\n\ts_nop 0\n\tglobal_load_lds_dwordx4 %0, %1" ::"v"(voff), "s"(sbase), "s"(lds) : "memory", "m0");
}
__device__ __forceinline__ const char* uni(const char* p) {
    unsigned long long v = (unsigned long long)p;
    unsigned lo = __builtin_amdgcn_readfirstlane((unsigned)v), hi = __builtin_amdgcn_readfirstlane((unsigned)(v >> 32));
    return (const char*)(((unsigned long long)hi << 32) | lo);
}
template <int N> __device__ __forceinline__ void wait_vm() { asm volatile("s_waitcnt vmcnt(%0)" ::"n"(N) : "memory"); }
__device__ __forceinline__ void bar_lgkm() {
    asm volatile("s_waitcnt lgkmcnt(0)" ::: "memory");
    __builtin_amdgcn_s_barrier();
    asm volatile("" ::: "memory");
}

__device__ __forceinline__ void chan_merge(float& n, float& m, float& q, float nb, float mb, float qb) {
    if (nb > 0.f) {
        float nt = n + nb;
        float d = mb - m;
        float r = nb * __builtin_amdgcn_rcpf(nt);
        m = m + d * r;
        q = q + qb + d * d * n * r;
        n = nt;
    }
}

__global__ __launch_bounds__(512, 2)
void conv_gn_gelu_kernel(const __hip_bfloat16* __restrict__ x, const __hip_bfloat16* __restrict__ w,
                         const __hip_bfloat16* __restrict__ bias, const __hip_bfloat16* __restrict__ gamma,
                         const __hip_bfloat16* __restrict__ beta, __hip_bfloat16* __restrict__ out,
                         unsigned long long* __restrict__ gran) {
    __shared__ __attribute__((aligned(16))) char smem[LDS_B];

    const int tid = threadIdx.x;
    const int lane = tid & 63;
    STAMP(0);
    const int wv8 = __builtin_amdgcn_readfirstlane(tid >> 6);
    const bool loader = wv8 >= 4;
    const int wv = wv8 & 3;
    const int wr = wv >> 1, wc = wv & 1;

    const int bid = blockIdx.x;
    const int xcd = bid & 7, idx = bid >> 3;
    const int mt = xcd * 4 + (idx >> 3);
    const int nt = idx & 7;
    const int batch = mt >> 4, mtb = mt & 15;
    const int l0 = mtb * BM, n0 = nt * BN;

    const char* xb = uni((const char*)(x + (size_t)batch * TT * DD));
    const char* wb = uni((const char*)w + n0 * 2);
    unsigned voff[15];
#pragma unroll
    for (int j = 0; j < 15; ++j) {
        if (j < 8 || j == 14) {
            int P = (j == 14) ? 16 : 4 * (j & 3) + wv;
            int par = (j >= 4 && j < 8) ? 1 : 0;
            int pos = 8 * P + (lane >> 3);
            int r = pos ^ (P & 1);
            int c = (lane & 7) ^ (r & 7);
            int row = 2 * (l0 + r) + par;
            row = row < TT ? row : TT - 1;
            voff[j] = (unsigned)row * (DD * 2) + c * 16;
        } else {
            int P = 4 * (j - 8) + wv;
            int kr = 8 * P + (lane >> 3);
            int tap = kr >> 6, kk = kr & 63;
            int c = (lane & 7) ^ (((kr >> 1) & 1) << 2);
            voff[j] = (unsigned)(tap * DD + kk) * (DD * 2) + c * 16;
        }
    }
    const unsigned lds_base = (unsigned)(size_t)(__attribute__((address_space(3))) char*)smem;
    auto piece = [&](int st, int j) {
        const unsigned lb = lds_base + (st & 1) * STAGE_B + wv * 1024;
        const char* xs = xb + st * (CK * 2);
        const char* ws = wb + (size_t)st * (CK * DD * 2);
        if (j < 4) glds16(voff[j], xs, lb + E_OFF + j * 4096);
        else if (j < 8) glds16(voff[j], xs, lb + O_OFF + (j - 4) * 4096);
        else if (j < 14) glds16(voff[j], ws, lb + W_OFF + (j - 8) * 4096);
        else { if (wv == 0) glds16(voff[14], xs, lb + E_OFF + 16 * 1024); }
    };
    constexpr int G1J[9] = {0, 1, 2, 3, 14, 8, 9, 12, 13};
    constexpr int G2J[6] = {4, 5, 6, 7, 10, 11};
    if (loader) {
#pragma unroll
        for (int k = 0; k < 9; ++k) piece(0, G1J[k]);
#pragma unroll
        for (int k = 0; k < 6; ++k) piece(0, G2J[k]);
#pragma unroll
        for (int k = 0; k < 9; ++k) piece(1, G1J[k]);
#pragma unroll
        for (int k = 0; k < 6; ++k) piece(1, G2J[k]);
        if (wv == 0) wait_vm<21>(); else wait_vm<20>();
        __builtin_amdgcn_s_barrier();
        if (wv == 0) wait_vm<15>(); else wait_vm<14>();
        for (int it = 0; it < NST; ++it) {
            const bool more = it + 2 < NST;
            __builtin_amdgcn_s_barrier();
            asm volatile("" ::: "memory");
            if (more) {
#pragma unroll
                for (int k = 0; k < 9; ++k) piece(it + 2, G1J[k]);
                if (wv == 0) wait_vm<15>(); else wait_vm<14>();
            } else if (it + 1 < NST) wait_vm<6>();
            __builtin_amdgcn_s_barrier();
            asm volatile("" ::: "memory");
            if (more) {
#pragma unroll
                for (int k = 0; k < 6; ++k) piece(it + 2, G2J[k]);
                if (wv == 0) wait_vm<15>(); else wait_vm<14>();
            } else wait_vm<0>();
        }
    }

    const int h = lane >> 5;
    int aoff[3][2];
#pragma unroll
    for (int mi = 0; mi < 2; ++mi) {
        int r = wr * 64 + mi * 32 + (lane & 31);
        int pr = r ^ ((r >> 3) & 1);
        aoff[0][mi] = E_OFF + pr * 128 + 16 * (h ^ (r & 7));
        aoff[1][mi] = O_OFF + pr * 128 + 16 * (h ^ (r & 7));
        int r1 = r + 1;
        int pr1 = r1 ^ ((r1 >> 3) & 1);
        aoff[2][mi] = E_OFF + pr1 * 128 + 16 * (h ^ (r1 & 7));
    }
    int boff;
    {
        int blk = (lane >> 4) & 1, q = (lane & 15) >> 2, p = lane & 3;
        int row = 8 * h + q;
        int ch = wc * 4 + 2 * blk + (p >> 1);
        boff = W_OFF + row * 128 + 16 * (ch ^ (((q >> 1) & 1) << 2)) + 8 * (p & 1);
    }
    const int col = wc * 32 + (lane & 31);
    const int gcol = n0 + col;
    const float bv = __bfloat162float(bias[gcol]);
    const float gcv = __bfloat162float(gamma[n0 + (tid & 63)]);
    const float bcv = __bfloat162float(beta[n0 + (tid & 63)]);

    f32x16 acc[2];
#pragma unroll
    for (int mi = 0; mi < 2; ++mi)
#pragma unroll
        for (int i = 0; i < 16; ++i) acc[mi][i] = 0.f;

    s16x8 a1[2][4][2], b1[2][4];
    s16x8 a2[4][2], b2[4];
    auto rd_b = [&](const char* sb, int tap, int s) {
        s16x4 b0 = __builtin_amdgcn_ds_read_tr16_b64_v4i16((lds_s16x4_p)(sb + boff + (tap * 64 + s * 16) * 128));
        s16x4 b1_ = __builtin_amdgcn_ds_read_tr16_b64_v4i16((lds_s16x4_p)(sb + boff + (tap * 64 + s * 16 + 4) * 128));
        return (s16x8){b0[0], b0[1], b0[2], b0[3], b1_[0], b1_[1], b1_[2], b1_[3]};
    };
    auto read_g1 = [&](int st) {
        const char* sb = smem + (st & 1) * STAGE_B;
#pragma unroll
        for (int t = 0; t < 2; ++t)
#pragma unroll
            for (int s = 0; s < 4; ++s) {
                b1[t][s] = rd_b(sb, 2 * t, s);
#pragma unroll
                for (int mi = 0; mi < 2; ++mi) a1[t][s][mi] = *(const s16x8*)(sb + (aoff[2 * t][mi] ^ (s * 32)));
            }
    };
    auto read_g2 = [&](int st) {
        const char* sb = smem + (st & 1) * STAGE_B;
#pragma unroll
        for (int s = 0; s < 4; ++s) {
            b2[s] = rd_b(sb, 1, s);
#pragma unroll
            for (int mi = 0; mi < 2; ++mi) a2[s][mi] = *(const s16x8*)(sb + (aoff[1][mi] ^ (s * 32)));
        }
    };
#define MMA(A, B, mi) acc[mi] = __builtin_amdgcn_mfma_f32_32x32x16_bf16(__builtin_bit_cast(bf16x8, A), __builtin_bit_cast(bf16x8, B), acc[mi], 0, 0, 0)
#define SB0 __builtin_amdgcn_sched_barrier(0)

    if (!loader) {
        bar_lgkm();
        read_g1(0);
        for (int it = 0; it < NST; ++it) {
            bar_lgkm();
            read_g2(it);
            SB0;
#pragma unroll
            for (int k = 0; k < 8; ++k) {
                MMA(a1[k >> 2][k & 3][0], b1[k >> 2][k & 3], 0);
                MMA(a1[k >> 2][k & 3][1], b1[k >> 2][k & 3], 1);
            }
            bar_lgkm();
            if (it + 1 < NST) read_g1(it + 1);
            SB0;
#pragma unroll
            for (int k = 0; k < 4; ++k) {
                MMA(a2[k][0], b2[k], 0);
                MMA(a2[k][1], b2[k], 1);
            }
        }
    }
    STAMP(1);

    const int nvalid = (LL - l0) < BM ? (LL - l0) : BM;
    float cnt, s1 = 0.f, s2 = 0.f;
    const float cshift = acc[0][0] + bv;
    if (loader) {
        cnt = 0.f;
    } else if (nvalid == BM) {
        cnt = 32.f;
#pragma unroll
        for (int mi = 0; mi < 2; ++mi)
#pragma unroll
            for (int i = 0; i < 16; ++i) {
                float v = acc[mi][i] + bv;
                acc[mi][i] = v;
                float d = v - cshift;
                s1 += d;
                s2 = fmaf(d, d, s2);
            }
    } else {
        cnt = 0.f;
#pragma unroll
        for (int mi = 0; mi < 2; ++mi)
#pragma unroll
            for (int i = 0; i < 16; ++i) {
                int row = wr * 64 + mi * 32 + (i & 3) + 8 * (i >> 2) + 4 * h;
                float v = acc[mi][i] + bv;
                acc[mi][i] = v;
                float d = row < nvalid ? v - cshift : 0.f;
                cnt += row < nvalid ? 1.f : 0.f;
                s1 += d;
                s2 = fmaf(d, d, s2);
            }
    }
    float mean_l = cshift, m2_l = 0.f;
    if (cnt > 0.f) { float ic = __builtin_amdgcn_rcpf(cnt); mean_l = cshift + s1 * ic; m2_l = s2 - s1 * s1 * ic; m2_l = m2_l > 0.f ? m2_l : 0.f; }
    {
        float nb = __shfl_xor(cnt, 32), mb = __shfl_xor(mean_l, 32), qb = __shfl_xor(m2_l, 32);
        float nt_ = cnt + nb;
        if (nt_ > 0.f) {
            float d = mb - mean_l;
            float rn = __builtin_amdgcn_rcpf(nt_);
            float mm = (cnt * mean_l + nb * mb) * rn;
            float qq = m2_l + qb + d * d * cnt * nb * rn;
            mean_l = mm; m2_l = qq; cnt = nt_;
        }
    }
    float* red = (float*)(smem + 32768);
    __syncthreads();
    if (h == 0 && !loader) {
        red[(wr * 64 + col) * 3 + 0] = cnt;
        red[(wr * 64 + col) * 3 + 1] = mean_l;
        red[(wr * 64 + col) * 3 + 2] = m2_l;
    }
    __syncthreads();
    gu64* gbase = (gu64*)(gran + ((size_t)(batch * 8 + nt) * 16) * 64);
    if (tid < 64) {
        float n = red[tid * 3], m = red[tid * 3 + 1], q = red[tid * 3 + 2];
        chan_merge(n, m, q, red[(64 + tid) * 3], red[(64 + tid) * 3 + 1], red[(64 + tid) * 3 + 2]);
        unsigned long long g = ((unsigned long long)(__float_as_uint(q) | 0x80000000u) << 32) | __float_as_uint(m);
        __hip_atomic_store(gbase + mtb * 64 + tid, g, __ATOMIC_RELAXED, __HIP_MEMORY_SCOPE_AGENT);
    }
    STAMP(2);
    float* yt = (float*)(smem + 40960);
    if (!loader) {
#pragma unroll
        for (int mi = 0; mi < 2; ++mi)
#pragma unroll
            for (int i = 0; i < 16; ++i) {
                int row = wr * 64 + mi * 32 + (i & 3) + 8 * (i >> 2) + 4 * h;
                yt[row * 68 + col] = acc[mi][i];
            }
    }
    float* gb = (float*)(smem + 32768);
    if (tid < 64) {
        unsigned long long g[16];
        for (;;) {
            bool ok = true;
#pragma unroll
            for (int j = 0; j < 16; ++j) {
                g[j] = __hip_atomic_load(gbase + j * 64 + tid, __ATOMIC_RELAXED, __HIP_MEMORY_SCOPE_AGENT);
                ok &= (g[j] >> 63) != 0;
            }
            if (__all(ok)) break;
            __builtin_amdgcn_s_sleep(1);
        }
        constexpr float NL = (float)(LL - 15 * BM), INVL = 1.f / (float)LL;
        float sm = 0.f;
#pragma unroll
        for (int j = 0; j < 16; ++j) sm = fmaf(__uint_as_float((unsigned)g[j]), j == 15 ? NL : (float)BM, sm);
        const float m = sm * INVL;
        float q = 0.f;
#pragma unroll
        for (int j = 0; j < 16; ++j) {
            float d = __uint_as_float((unsigned)g[j]) - m;
            q += __uint_as_float((unsigned)(g[j] >> 32) & 0x7fffffffu);
            q = fmaf(d * d, j == 15 ? NL : (float)BM, q);
        }
        float rstd = __builtin_amdgcn_rsqf(fmaf(q, INVL, 1e-5f));
        float ga = gcv * rstd;
        gb[tid] = ga;
        gb[64 + tid] = bcv - m * ga;
    }
    __syncthreads();
    STAMP(3);
    {
        constexpr float K1 = -2.f * 0.7978845608028654f * 1.4426950408889634f;
        constexpr float K2 = K1 * 0.044715f;
        const int ch = tid & 7;
        float ga[8], be[8];
#pragma unroll
        for (int k = 0; k < 2; ++k) {
            float4 t0 = *(const float4*)(gb + ch * 8 + 4 * k);
            float4 t1 = *(const float4*)(gb + 64 + ch * 8 + 4 * k);
            ga[4 * k] = t0.x; ga[4 * k + 1] = t0.y; ga[4 * k + 2] = t0.z; ga[4 * k + 3] = t0.w;
            be[4 * k] = t1.x; be[4 * k + 1] = t1.y; be[4 * k + 2] = t1.z; be[4 * k + 3] = t1.w;
        }
#pragma unroll
        for (int j = 0; j < 2; ++j) {
            const int row = (tid >> 3) + 64 * j;
            float y[8];
#pragma unroll
            for (int k = 0; k < 2; ++k) {
                float4 t = *(const float4*)(yt + row * 68 + ch * 8 + 4 * k);
                y[4 * k] = t.x; y[4 * k + 1] = t.y; y[4 * k + 2] = t.z; y[4 * k + 3] = t.w;
            }
            unsigned pk[4];
#pragma unroll
            for (int k = 0; k < 4; ++k) {
                float o2[2];
#pragma unroll
                for (int u = 0; u < 2; ++u) {
                    float v = fmaf(y[2 * k + u], ga[2 * k + u], be[2 * k + u]);
                    float t = v * fmaf(v * v, K2, K1);
                    float e = __builtin_amdgcn_exp2f(t);
                    o2[u] = v * __builtin_amdgcn_rcpf(1.f + e);
                }
                __hip_bfloat16 lo = __float2bfloat16(o2[0]), hi = __float2bfloat16(o2[1]);
                pk[k] = (unsigned)*(unsigned short*)&lo | ((unsigned)*(unsigned short*)&hi << 16);
            }
            if (row < nvalid)
                *(uint4*)(out + ((size_t)(batch * LL + l0 + row) * DD + n0 + ch * 8)) = make_uint4(pk[0], pk[1], pk[2], pk[3]);
        }
    }
    STAMP(4);
}

extern "C" void kernel_launch(void* const* d_in, const int* in_sizes, int n_in,
                              void* d_out, int out_size, void* d_ws, size_t ws_size,
                              hipStream_t stream) {
    const __hip_bfloat16* x = (const __hip_bfloat16*)d_in[0];
    const __hip_bfloat16* w = (const __hip_bfloat16*)d_in[1];
    const __hip_bfloat16* b = (const __hip_bfloat16*)d_in[2];
    const __hip_bfloat16* gamma = (const __hip_bfloat16*)d_in[3];
    const __hip_bfloat16* beta = (const __hip_bfloat16*)d_in[4];
    __hip_bfloat16* out = (__hip_bfloat16*)d_out;
    unsigned long long* gran = (unsigned long long*)d_ws;
    (void)hipMemsetAsync(d_ws, 0, 2 * 8 * 16 * 64 * 8, stream);
    conv_gn_gelu_kernel<<<256, 512, 0, stream>>>(x, w, b, gamma, beta, out, gran);
}
```

```cpp
#include <hip/hip_runtime.h>
#include <hip/hip_bf16.h>

typedef __attribute__((ext_vector_type(8))) short s16x8;
typedef __attribute__((ext_vector_type(4))) short s16x4;
typedef __attribute__((ext_vector_type(8))) __bf16 bf16x8;
typedef __attribute__((ext_vector_type(16))) float f32x16;
typedef __attribute__((address_space(3))) s16x4* lds_s16x4_p;
typedef __attribute__((address_space(1))) unsigned long long gu64;

constexpr int TT = 4000, DD = 512, LL = 1999;
constexpr int BM = 128, BN = 64;
constexpr int CK = 64;
constexpr int NST = DD / CK;
constexpr int E_OFF = 0, O_OFF = 136 * 128, W_OFF = O_OFF + 128 * 128;
constexpr int STAGE_B = W_OFF + 192 * 128;
constexpr int LDS_B = 2 * STAGE_B;

#define STAMP(i)

__device__ __forceinline__ void glds16(unsigned voff, const void* sbase, unsigned lds) {
    asm volatile("s_mov_b32 m0, %2\n\ts_nop 0\n\tglobal_load_lds_dwordx4 %0, %1" ::"v"(voff), "s"(sbase), "s"(lds) : "memory", "m0");
}
__device__ __forceinline__ const char* uni(const char* p) {
    unsigned long long v = (unsigned long long)p;
    unsigned lo = __builtin_amdgcn_readfirstlane((unsigned)v), hi = __builtin_amdgcn_readfirstlane((unsigned)(v >> 32));
    return (const char*)(((unsigned long long)hi << 32) | lo);
}
template <int N> __device__ __forceinline__ void wait_vm() { asm volatile("s_waitcnt vmcnt(%0)" ::"n"(N) : "memory"); }
__device__ __forceinline__ void bar_lgkm() {
    asm volatile("s_waitcnt lgkmcnt(0)" ::: "memory");
    __builtin_amdgcn_s_barrier();
    asm volatile("" ::: "memory");
}

__device__ __forceinline__ void chan_merge(float& n, float& m, float& q, float nb, float mb, float qb) {
    if (nb > 0.f) {
        float nt = n + nb;
        float d = mb - m;
        float r = nb * __builtin_amdgcn_rcpf(nt);
        m = m + d * r;
        q = q + qb + d * d * n * r;
        n = nt;
    }
}

__global__ __launch_bounds__(512, 2)
void conv_gn_gelu_kernel(const __hip_bfloat16* __restrict__ x, const __hip_bfloat16* __restrict__ w,
                         const __hip_bfloat16* __restrict__ bias, const __hip_bfloat16* __restrict__ gamma,
                         const __hip_bfloat16* __restrict__ beta, __hip_bfloat16* __restrict__ out,
                         unsigned long long* __restrict__ gran) {
    __shared__ __attribute__((aligned(16))) char smem[LDS_B];

    const int tid = threadIdx.x;
    const int lane = tid & 63;
    STAMP(0);
    const int wv8 = __builtin_amdgcn_readfirstlane(tid >> 6);
    const bool loader = wv8 >= 4;
    const int wv = wv8 & 3;
    const int wr = wv >> 1, wc = wv & 1;

    const int bid = blockIdx.x;
    const int xcd = bid & 7, idx = bid >> 3;
    const int mt = xcd * 4 + (idx >> 3);
    const int nt = idx & 7;
    const int batch = mt >> 4, mtb = mt & 15;
    const int l0 = mtb * BM, n0 = nt * BN;

    const char* xb = uni((const char*)(x + (size_t)batch * TT * DD));
    const char* wb = uni((const char*)w + n0 * 2);
    unsigned voff[15];
#pragma unroll
    for (int j = 0; j < 15; ++j) {
        if (j < 8 || j == 14) {
            int P = (j == 14) ? 16 : 4 * (j & 3) + wv;
            int par = (j >= 4 && j < 8) ? 1 : 0;
            int pos = 8 * P + (lane >> 3);
            int r = pos ^ (P & 1);
            int c = (lane & 7) ^ (r & 7);
            int row = 2 * (l0 + r) + par;
            row = row < TT ? row : TT - 1;
            voff[j] = (unsigned)row * (DD * 2) + c * 16;
        } else {
            int P = 4 * (j - 8) + wv;
            int kr = 8 * P + (lane >> 3);
            int tap = kr >> 6, kk = kr & 63;
            int c = (lane & 7) ^ (((kr >> 1) & 1) << 2);
            voff[j] = (unsigned)(tap * DD + kk) * (DD * 2) + c * 16;
        }
    }
    const unsigned lds_base = (unsigned)(size_t)(__attribute__((address_space(3))) char*)smem;
    auto piece = [&](int st, int j) {
        const unsigned lb = lds_base + (st & 1) * STAGE_B + wv * 1024;
        const char* xs = xb + st * (CK * 2);
        const char* ws = wb + (size_t)st * (CK * DD * 2);
        if (j < 4) glds16(voff[j], xs, lb + E_OFF + j * 4096);
        else if (j < 8) glds16(voff[j], xs, lb + O_OFF + (j - 4) * 4096);
        else if (j < 14) glds16(voff[j], ws, lb + W_OFF + (j - 8) * 4096);
        else { if (wv == 0) glds16(voff[14], xs, lb + E_OFF + 16 * 1024); }
    };
    constexpr int G1J[9] = {0, 1, 2, 3, 14, 8, 9, 12, 13};
    constexpr int G2J[6] = {4, 5, 6, 7, 10, 11};
    if (loader) {
#pragma unroll
        for (int k = 0; k < 9; ++k) piece(0, G1J[k]);
#pragma unroll
        for (int k = 0; k < 6; ++k) piece(0, G2J[k]);
#pragma unroll
        for (int k = 0; k < 9; ++k) piece(1, G1J[k]);
#pragma unroll
        for (int k = 0; k < 6; ++k) piece(1, G2J[k]);
        if (wv == 0) wait_vm<21>(); else wait_vm<20>();
        __builtin_amdgcn_s_barrier();
        if (wv == 0) wait_vm<15>(); else wait_vm<14>();
        for (int it = 0; it < NST; ++it) {
            const bool more = it + 2 < NST;
            __builtin_amdgcn_s_barrier();
            asm volatile("" ::: "memory");
            if (more) {
#pragma unroll
                for (int k = 0; k < 9; ++k) piece(it + 2, G1J[k]);
                if (wv == 0) wait_vm<15>(); else wait_vm<14>();
            } else if (it + 1 < NST) wait_vm<6>();
            __builtin_amdgcn_s_barrier();
            asm volatile("" ::: "memory");
            if (more) {
#pragma unroll
                for (int k = 0; k < 6; ++k) piece(it + 2, G2J[k]);
                if (wv == 0) wait_vm<15>(); else wait_vm<14>();
            } else wait_vm<0>();
        }
    }

    const int h = lane >> 5;
    int aoff[3][2];
#pragma unroll
    for (int mi = 0; mi < 2; ++mi) {
        int r = wr * 64 + mi * 32 + (lane & 31);
        int pr = r ^ ((r >> 3) & 1);
        aoff[0][mi] = E_OFF + pr * 128 + 16 * (h ^ (r & 7));
        aoff[1][mi] = O_OFF + pr * 128 + 16 * (h ^ (r & 7));
        int r1 = r + 1;
        int pr1 = r1 ^ ((r1 >> 3) & 1);
        aoff[2][mi] = E_OFF + pr1 * 128 + 16 * (h ^ (r1 & 7));
    }
    int boff;
    {
        int blk = (lane >> 4) & 1, q = (lane & 15) >> 2, p = lane & 3;
        int row = 8 * h + q;
        int ch = wc * 4 + 2 * blk + (p >> 1);
        boff = W_OFF + row * 128 + 16 * (ch ^ (((q >> 1) & 1) << 2)) + 8 * (p & 1);
    }
    const int col = wc * 32 + (lane & 31);
    const int gcol = n0 + col;
    const float bv = __bfloat162float(bias[gcol]);
    const float gcv = __bfloat162float(gamma[n0 + (tid & 63)]);
    const float bcv = __bfloat162float(beta[n0 + (tid & 63)]);

    f32x16 acc[2];
#pragma unroll
    for (int mi = 0; mi < 2; ++mi)
#pragma unroll
        for (int i = 0; i < 16; ++i) acc[mi][i] = 0.f;

    s16x8 a1[2][4][2], b1[2][4];
    s16x8 a2[4][2], b2[4];
    auto rd_b = [&](const char* sb, int tap, int s) {
        s16x4 b0 = __builtin_amdgcn_ds_read_tr16_b64_v4i16((lds_s16x4_p)(sb + boff + (tap * 64 + s * 16) * 128));
        s16x4 b1_ = __builtin_amdgcn_ds_read_tr16_b64_v4i16((lds_s16x4_p)(sb + boff + (tap * 64 + s * 16 + 4) * 128));
        return (s16x8){b0[0], b0[1], b0[2], b0[3], b1_[0], b1_[1], b1_[2], b1_[3]};
    };
    auto read_g1 = [&](int st) {
        const char* sb = smem + (st & 1) * STAGE_B;
#pragma unroll
        for (int t = 0; t < 2; ++t)
#pragma unroll
            for (int s = 0; s < 4; ++s) {
                b1[t][s] = rd_b(sb, 2 * t, s);
#pragma unroll
                for (int mi = 0; mi < 2; ++mi) a1[t][s][mi] = *(const s16x8*)(sb + (aoff[2 * t][mi] ^ (s * 32)));
            }
    };
    auto read_g2 = [&](int st) {
        const char* sb = smem + (st & 1) * STAGE_B;
#pragma unroll
        for (int s = 0; s < 4; ++s) {
            b2[s] = rd_b(sb, 1, s);
#pragma unroll
            for (int mi = 0; mi < 2; ++mi) a2[s][mi] = *(const s16x8*)(sb + (aoff[1][mi] ^ (s * 32)));
        }
    };
#define MMA(A, B, mi) acc[mi] = __builtin_amdgcn_mfma_f32_32x32x16_bf16(__builtin_bit_cast(bf16x8, A), __builtin_bit_cast(bf16x8, B), acc[mi], 0, 0, 0)
#define SB0 __builtin_amdgcn_sched_barrier(0)

    if (!loader) {
        bar_lgkm();
        read_g1(0);
        for (int it = 0; it < NST; ++it) {
            bar_lgkm();
            read_g2(it);
            SB0;
#pragma unroll
            for (int k = 0; k < 8; ++k) {
                MMA(a1[k >> 2][k & 3][0], b1[k >> 2][k & 3], 0);
                MMA(a1[k >> 2][k & 3][1], b1[k >> 2][k & 3], 1);
            }
            bar_lgkm();
            if (it + 1 < NST) read_g1(it + 1);
            SB0;
#pragma unroll
            for (int k = 0; k < 4; ++k) {
                MMA(a2[k][0], b2[k], 0);
                MMA(a2[k][1], b2[k], 1);
            }
        }
    }
    STAMP(1);

    const int nvalid = (LL - l0) < BM ? (LL - l0) : BM;
    float cnt, s1 = 0.f, s2 = 0.f;
    const float cshift = acc[0][0] + bv;
    if (loader) {
        cnt = 0.f;
    } else if (nvalid == BM) {
        cnt = 32.f;
#pragma unroll
        for (int mi = 0; mi < 2; ++mi)
#pragma unroll
            for (int i = 0; i < 16; ++i) {
                float v = acc[mi][i] + bv;
                acc[mi][i] = v;
                float d = v - cshift;
                s1 += d;
                s2 = fmaf(d, d, s2);
            }
    } else {
        cnt = 0.f;
#pragma unroll
        for (int mi = 0; mi < 2; ++mi)
#pragma unroll
            for (int i = 0; i < 16; ++i) {
                int row = wr * 64 + mi * 32 + (i & 3) + 8 * (i >> 2) + 4 * h;
                float v = acc[mi][i] + bv;
                acc[mi][i] = v;
                float d = row < nvalid ? v - cshift : 0.f;
                cnt += row < nvalid ? 1.f : 0.f;
                s1 += d;
                s2 = fmaf(d, d, s2);
            }
    }
    float mean_l = cshift, m2_l = 0.f;
    if (cnt > 0.f) { float ic = __builtin_amdgcn_rcpf(cnt); mean_l = cshift + s1 * ic; m2_l = s2 - s1 * s1 * ic; m2_l = m2_l > 0.f ? m2_l : 0.f; }
    {
        float nb = __shfl_xor(cnt, 32), mb = __shfl_xor(mean_l, 32), qb = __shfl_xor(m2_l, 32);
        float nt_ = cnt + nb;
        if (nt_ > 0.f) {
            float d = mb - mean_l;
            float rn = __builtin_amdgcn_rcpf(nt_);
            float mm = (cnt * mean_l + nb * mb) * rn;
            float qq = m2_l + qb + d * d * cnt * nb * rn;
            mean_l = mm; m2_l = qq; cnt = nt_;
        }
    }
    float* red = (float*)(smem + 32768);
    __syncthreads();
    if (h == 0 && !loader) {
        red[(wr * 64 + col) * 3 + 0] = cnt;
        red[(wr * 64 + col) * 3 + 1] = mean_l;
        red[(wr * 64 + col) * 3 + 2] = m2_l;
    }
    __syncthreads();
    gu64* gbase = (gu64*)(gran + ((size_t)(batch * 8 + nt) * 16) * 64);
    if (tid < 64) {
        float n = red[tid * 3], m = red[tid * 3 + 1], q = red[tid * 3 + 2];
        chan_merge(n, m, q, red[(64 + tid) * 3], red[(64 + tid) * 3 + 1], red[(64 + tid) * 3 + 2]);
        unsigned long long g = ((unsigned long long)(__float_as_uint(q) | 0x80000000u) << 32) | __float_as_uint(m);
        __hip_atomic_store(gbase + mtb * 64 + tid, g, __ATOMIC_RELAXED, __HIP_MEMORY_SCOPE_AGENT);
    }
    STAMP(2);
    float* yt = (float*)(smem + 40960);
    if (!loader) {
#pragma unroll
        for (int mi = 0; mi < 2; ++mi)
#pragma unroll
            for (int i = 0; i < 16; ++i) {
                int row = wr * 64 + mi * 32 + (i & 3) + 8 * (i >> 2) + 4 * h;
                yt[row * 68 + col] = acc[mi][i];
            }
    }
    float* gb = (float*)(smem + 32768);
    if (tid < 64) {
        unsigned long long g[16];
        for (;;) {
            bool ok = true;
#pragma unroll
            for (int j = 0; j < 16; ++j) {
                g[j] = __hip_atomic_load(gbase + j * 64 + tid, __ATOMIC_RELAXED, __HIP_MEMORY_SCOPE_AGENT);
                ok &= (g[j] >> 63) != 0;
            }
            if (__all(ok)) break;
            __builtin_amdgcn_s_sleep(1);
        }
        constexpr float NL = (float)(LL - 15 * BM), INVL = 1.f / (float)LL;
        float sm = 0.f;
#pragma unroll
        for (int j = 0; j < 16; ++j) sm = fmaf(__uint_as_float((unsigned)g[j]), j == 15 ? NL : (float)BM, sm);
        const float m = sm * INVL;
        float q = 0.f;
#pragma unroll
        for (int j = 0; j < 16; ++j) {
            float d = __uint_as_float((unsigned)g[j]) - m;
            q += __uint_as_float((unsigned)(g[j] >> 32) & 0x7fffffffu);
            q = fmaf(d * d, j == 15 ? NL : (float)BM, q);
        }
        float rstd = __builtin_amdgcn_rsqf(fmaf(q, INVL, 1e-5f));
        float ga = gcv * rstd;
        gb[tid] = ga;
        gb[64 + tid] = bcv - m * ga;
    }
    __syncthreads();
    STAMP(3);
    {
        constexpr float K1 = -2.f * 0.7978845608028654f * 1.4426950408889634f;
        constexpr float K2 = K1 * 0.044715f;
        const int ch = tid & 7;
        float ga[8], be[8];
#pragma unroll
        for (int k = 0; k < 2; ++k) {
            float4 t0 = *(const float4*)(gb + ch * 8 + 4 * k);
            float4 t1 = *(const float4*)(gb + 64 + ch * 8 + 4 * k);
            ga[4 * k] = t0.x; ga[4 * k + 1] = t0.y; ga[4 * k + 2] = t0.z; ga[4 * k + 3] = t0.w;
            be[4 * k] = t1.x; be[4 * k + 1] = t1.y; be[4 * k + 2] = t1.z; be[4 * k + 3] = t1.w;
        }
#pragma unroll
        for (int j = 0; j < 2; ++j) {
            const int row = (tid >> 3) + 64 * j;
            float y[8];
#pragma unroll
            for (int k = 0; k < 2; ++k) {
                float4 t = *(const float4*)(yt + row * 68 + ch * 8 + 4 * k);
                y[4 * k] = t.x; y[4 * k + 1] = t.y; y[4 * k + 2] = t.z; y[4 * k + 3] = t.w;
            }
            unsigned pk[4];
#pragma unroll
            for (int k = 0; k < 4; ++k) {
                float o2[2];
#pragma unroll
                for (int u = 0; u < 2; ++u) {
                    float v = fmaf(y[2 * k + u], ga[2 * k + u], be[2 * k + u]);
                    float t = v * fmaf(v * v, K2, K1);
                    float e = __builtin_amdgcn_exp2f(t);
                    o2[u] = v * __builtin_amdgcn_rcpf(1.f + e);
                }
                __hip_bfloat16 lo = __float2bfloat16(o2[0]), hi = __float2bfloat16(o2[1]);
                pk[k] = (unsigned)*(unsigned short*)&lo | ((unsigned)*(unsigned short*)&hi << 16);
            }
            if (row < nvalid)
                *(uint4*)(out + ((size_t)(batch * LL + l0 + row) * DD + n0 + ch * 8)) = make_uint4(pk[0], pk[1], pk[2], pk[3]);
        }
    }
    STAMP(4);
}

__global__ __launch_bounds__(256) void prep_kernel(uint4* __restrict__ flags, const uint4* __restrict__ x, const uint4* __restrict__ w) {
    const int g = blockIdx.x * 256 + threadIdx.x;
    if (g < 8192) flags[g] = make_uint4(0u, 0u, 0u, 0u);
    uint4 a[10];
#pragma unroll
    for (int j = 0; j < 8; ++j) { int i = g + j * 65536; a[j] = x[i < 512000 ? i : 0]; }
    a[8] = w[g];
    a[9] = w[(g & 32767) + 65536];
    unsigned r = 0;
#pragma unroll
    for (int j = 0; j < 10; ++j) r ^= a[j].x ^ a[j].y ^ a[j].z ^ a[j].w;
    asm volatile("" ::"v"(r));
}

extern "C" void kernel_launch(void* const* d_in, const int* in_sizes, int n_in,
                              void* d_out, int out_size, void* d_ws, size_t ws_size,
                              hipStream_t stream) {
    const __hip_bfloat16* x = (const __hip_bfloat16*)d_in[0];
    const __hip_bfloat16* w = (const __hip_bfloat16*)d_in[1];
    const __hip_bfloat16* b = (const __hip_bfloat16*)d_in[2];
    const __hip_bfloat16* gamma = (const __hip_bfloat16*)d_in[3];
    const __hip_bfloat16* beta = (const __hip_bfloat16*)d_in[4];
    __hip_bfloat16* out = (__hip_bfloat16*)d_out;
    unsigned long long* gran = (unsigned long long*)d_ws;
    prep_kernel<<<256, 256, 0, stream>>>((uint4*)d_ws, (const uint4*)x, (const uint4*)w);
    conv_gn_gelu_kernel<<<256, 512, 0, stream>>>(x, w, b, gamma, beta, out, gran);
}
```

```cpp
#include <hip/hip_runtime.h>
#include <hip/hip_bf16.h>

typedef __attribute__((ext_vector_type(8))) short s16x8;
typedef __attribute__((ext_vector_type(4))) short s16x4;
typedef __attribute__((ext_vector_type(8))) __bf16 bf16x8;
typedef __attribute__((ext_vector_type(16))) float f32x16;
typedef __attribute__((address_space(3))) s16x4* lds_s16x4_p;
typedef __attribute__((address_space(1))) unsigned long long gu64;

constexpr int TT = 4000, DD = 512, LL = 1999;
constexpr int BM = 128, BN = 64;
constexpr int CK = 64;
constexpr int NST = DD / CK;
constexpr int E_OFF = 0, O_OFF = 136 * 128, W_OFF = O_OFF + 128 * 128;
constexpr int STAGE_B = W_OFF + 192 * 128;
constexpr int LDS_B = 2 * STAGE_B;


__device__ __forceinline__ void glds16(unsigned voff, const void* sbase, unsigned lds) {
    asm volatile("s_mov_b32 m0, %2\n\ts_nop 0\n\tglobal_load_lds_dwordx4 %0, %1" ::"v"(voff), "s"(sbase), "s"(lds) : "memory", "m0");
}
__device__ __forceinline__ const char* uni(const char* p) {
    unsigned long long v = (unsigned long long)p;
    unsigned lo = __builtin_amdgcn_readfirstlane((unsigned)v), hi = __builtin_amdgcn_readfirstlane((unsigned)(v >> 32));
    return (const char*)(((unsigned long long)hi << 32) | lo);
}
template <int N> __device__ __forceinline__ void wait_vm() { asm volatile("s_waitcnt vmcnt(%0)" ::"n"(N) : "memory"); }
__device__ __forceinline__ void bar_lgkm() {
    asm volatile("s_waitcnt lgkmcnt(0)" ::: "memory");
    __builtin_amdgcn_s_barrier();
    asm volatile("" ::: "memory");
}

__device__ __forceinline__ void chan_merge(float& n, float& m, float& q, float nb, float mb, float qb) {
    if (nb > 0.f) {
        float nt = n + nb;
        float d = mb - m;
        float r = nb * __builtin_amdgcn_rcpf(nt);
        m = m + d * r;
        q = q + qb + d * d * n * r;
        n = nt;
    }
}


__global__ __launch_bounds__(512, 2)
void conv_gn_gelu_kernel(const __hip_bfloat16* __restrict__ x, const __hip_bfloat16* __restrict__ w,
                         const __hip_bfloat16* __restrict__ bias, const __hip_bfloat16* __restrict__ gamma,
                         const __hip_bfloat16* __restrict__ beta, __hip_bfloat16* __restrict__ out,
                         unsigned long long* __restrict__ gran) {
    __shared__ __attribute__((aligned(16))) char smem[LDS_B];

    const int tid = threadIdx.x;
    const int lane = tid & 63;
    const int wv8 = __builtin_amdgcn_readfirstlane(tid >> 6);
    const bool loader = wv8 >= 4;
    const int wv = wv8 & 3;
    const int wr = wv >> 1, wc = wv & 1;

    const int bid = blockIdx.x;
    const int xcd = bid & 7, idx = bid >> 3;
    const int mt = xcd * 4 + (idx >> 3);
    const int nt = idx & 7;
    const int batch = mt >> 4, mtb = mt & 15;
    const int l0 = mtb * BM, n0 = nt * BN;

    const char* xb = uni((const char*)(x + (size_t)batch * TT * DD));
    const char* wb = uni((const char*)w + n0 * 2);
    unsigned voff[15];
#pragma unroll
    for (int j = 0; j < 15; ++j) {
        if (j < 8 || j == 14) {
            int P = (j == 14) ? 16 : 4 * (j & 3) + wv;
            int par = (j >= 4 && j < 8) ? 1 : 0;
            int pos = 8 * P + (lane >> 3);
            int r = pos ^ (P & 1);
            int c = (lane & 7) ^ (r & 7);
            int row = 2 * (l0 + r) + par;
            row = row < TT ? row : TT - 1;
            voff[j] = (unsigned)row * (DD * 2) + c * 16;
        } else {
            int P = 4 * (j - 8) + wv;
            int kr = 8 * P + (lane >> 3);
            int tap = kr >> 6, kk = kr & 63;
            int c = (lane & 7) ^ (((kr >> 1) & 1) << 2);
            voff[j] = (unsigned)(tap * DD + kk) * (DD * 2) + c * 16;
        }
    }
    const unsigned lds_base = (unsigned)(size_t)(__attribute__((address_space(3))) char*)smem;
    auto piece = [&](int st, int j) {
        const unsigned lb = lds_base + (st & 1) * STAGE_B + wv * 1024;
        const char* xs = xb + st * (CK * 2);
        const char* ws = wb + (size_t)st * (CK * DD * 2);
        if (j < 4) glds16(voff[j], xs, lb + E_OFF + j * 4096);
        else if (j < 8) glds16(voff[j], xs, lb + O_OFF + (j - 4) * 4096);
        else if (j < 14) glds16(voff[j], ws, lb + W_OFF + (j - 8) * 4096);
        else { if (wv == 0) glds16(voff[14], xs, lb + E_OFF + 16 * 1024); }
    };
    constexpr int G1J[9] = {0, 1, 2, 3, 14, 8, 9, 12, 13};
    constexpr int G2J[6] = {4, 5, 6, 7, 10, 11};
    if (loader) {
#pragma unroll
        for (int k = 0; k < 9; ++k) piece(0, G1J[k]);
#pragma unroll
        for (int k = 0; k < 6; ++k) piece(0, G2J[k]);
#pragma unroll
        for (int k = 0; k < 9; ++k) piece(1, G1J[k]);
#pragma unroll
        for (int k = 0; k < 6; ++k) piece(1, G2J[k]);
        if (wv == 0) wait_vm<21>(); else wait_vm<20>();
        __builtin_amdgcn_s_barrier();
        if (wv == 0) wait_vm<15>(); else wait_vm<14>();
        for (int it = 0; it < NST; ++it) {
            const bool more = it + 2 < NST;
            __builtin_amdgcn_s_barrier();
            asm volatile("" ::: "memory");
            if (more) {
#pragma unroll
                for (int k = 0; k < 9; ++k) piece(it + 2, G1J[k]);
                if (wv == 0) wait_vm<15>(); else wait_vm<14>();
            } else if (it + 1 < NST) wait_vm<6>();
            __builtin_amdgcn_s_barrier();
            asm volatile("" ::: "memory");
            if (more) {
#pragma unroll
                for (int k = 0; k < 6; ++k) piece(it + 2, G2J[k]);
                if (wv == 0) wait_vm<15>(); else wait_vm<14>();
            } else wait_vm<0>();
        }
    }

    const int h = lane >> 5;
    int aoff[3][2];
#pragma unroll
    for (int mi = 0; mi < 2; ++mi) {
        int r = wr * 64 + mi * 32 + (lane & 31);
        int pr = r ^ ((r >> 3) & 1);
        aoff[0][mi] = E_OFF + pr * 128 + 16 * (h ^ (r & 7));
        aoff[1][mi] = O_OFF + pr * 128 + 16 * (h ^ (r & 7));
        int r1 = r + 1;
        int pr1 = r1 ^ ((r1 >> 3) & 1);
        aoff[2][mi] = E_OFF + pr1 * 128 + 16 * (h ^ (r1 & 7));
    }
    int boff;
    {
        int blk = (lane >> 4) & 1, q = (lane & 15) >> 2, p = lane & 3;
        int row = 8 * h + q;
        int ch = wc * 4 + 2 * blk + (p >> 1);
        boff = W_OFF + row * 128 + 16 * (ch ^ (((q >> 1) & 1) << 2)) + 8 * (p & 1);
    }
    const int col = wc * 32 + (lane & 31);
    const int gcol = n0 + col;
    const float bv = __bfloat162float(bias[gcol]);
    const float gcv = __bfloat162float(gamma[n0 + (tid & 63)]);
    const float bcv = __bfloat162float(beta[n0 + (tid & 63)]);

    f32x16 acc[2];
#pragma unroll
    for (int mi = 0; mi < 2; ++mi)
#pragma unroll
        for (int i = 0; i < 16; ++i) acc[mi][i] = 0.f;

    s16x8 a1[2][4][2], b1[2][4];
    s16x8 a2[4][2], b2[4];
    auto rd_b = [&](const char* sb, int tap, int s) {
        s16x4 b0 = __builtin_amdgcn_ds_read_tr16_b64_v4i16((lds_s16x4_p)(sb + boff + (tap * 64 + s * 16) * 128));
        s16x4 b1_ = __builtin_amdgcn_ds_read_tr16_b64_v4i16((lds_s16x4_p)(sb + boff + (tap * 64 + s * 16 + 4) * 128));
        return (s16x8){b0[0], b0[1], b0[2], b0[3], b1_[0], b1_[1], b1_[2], b1_[3]};
    };
    auto read_g1 = [&](int st) {
        const char* sb = smem + (st & 1) * STAGE_B;
#pragma unroll
        for (int t = 0; t < 2; ++t)
#pragma unroll
            for (int s = 0; s < 4; ++s) {
                b1[t][s] = rd_b(sb, 2 * t, s);
#pragma unroll
                for (int mi = 0; mi < 2; ++mi) a1[t][s][mi] = *(const s16x8*)(sb + (aoff[2 * t][mi] ^ (s * 32)));
            }
    };
    auto read_g2 = [&](int st) {
        const char* sb = smem + (st & 1) * STAGE_B;
#pragma unroll
        for (int s = 0; s < 4; ++s) {
            b2[s] = rd_b(sb, 1, s);
#pragma unroll
            for (int mi = 0; mi < 2; ++mi) a2[s][mi] = *(const s16x8*)(sb + (aoff[1][mi] ^ (s * 32)));
        }
    };
#define MMA(A, B, mi) acc[mi] = __builtin_amdgcn_mfma_f32_32x32x16_bf16(__builtin_bit_cast(bf16x8, A), __builtin_bit_cast(bf16x8, B), acc[mi], 0, 0, 0)
#define SB0 __builtin_amdgcn_sched_barrier(0)

    if (!loader) {
        bar_lgkm();
        read_g1(0);
        for (int it = 0; it < NST; ++it) {
            bar_lgkm();
            read_g2(it);
            SB0;
#pragma unroll
            for (int k = 0; k < 8; ++k) {
                MMA(a1[k >> 2][k & 3][0], b1[k >> 2][k & 3], 0);
                MMA(a1[k >> 2][k & 3][1], b1[k >> 2][k & 3], 1);
            }
            bar_lgkm();
            if (it + 1 < NST) read_g1(it + 1);
            SB0;
#pragma unroll
            for (int k = 0; k < 4; ++k) {
                MMA(a2[k][0], b2[k], 0);
                MMA(a2[k][1], b2[k], 1);
            }
        }
    }

    const int nvalid = (LL - l0) < BM ? (LL - l0) : BM;
    float cnt, s1 = 0.f, s2 = 0.f;
    const float cshift = acc[0][0] + bv;
    if (loader) {
        cnt = 0.f;
    } else if (nvalid == BM) {
        cnt = 32.f;
#pragma unroll
        for (int mi = 0; mi < 2; ++mi)
#pragma unroll
            for (int i = 0; i < 16; ++i) {
                float v = acc[mi][i] + bv;
                acc[mi][i] = v;
                float d = v - cshift;
                s1 += d;
                s2 = fmaf(d, d, s2);
            }
    } else {
        cnt = 0.f;
#pragma unroll
        for (int mi = 0; mi < 2; ++mi)
#pragma unroll
            for (int i = 0; i < 16; ++i) {
                int row = wr * 64 + mi * 32 + (i & 3) + 8 * (i >> 2) + 4 * h;
                float v = acc[mi][i] + bv;
                acc[mi][i] = v;
                float d = row < nvalid ? v - cshift : 0.f;
                cnt += row < nvalid ? 1.f : 0.f;
                s1 += d;
                s2 = fmaf(d, d, s2);
            }
    }
    float mean_l = cshift, m2_l = 0.f;
    if (cnt > 0.f) { float ic = __builtin_amdgcn_rcpf(cnt); mean_l = cshift + s1 * ic; m2_l = s2 - s1 * s1 * ic; m2_l = m2_l > 0.f ? m2_l : 0.f; }
    {
        float nb = __shfl_xor(cnt, 32), mb = __shfl_xor(mean_l, 32), qb = __shfl_xor(m2_l, 32);
        float nt_ = cnt + nb;
        if (nt_ > 0.f) {
            float d = mb - mean_l;
            float rn = __builtin_amdgcn_rcpf(nt_);
            float mm = (cnt * mean_l + nb * mb) * rn;
            float qq = m2_l + qb + d * d * cnt * nb * rn;
            mean_l = mm; m2_l = qq; cnt = nt_;
        }
    }
    float* red = (float*)(smem + 32768);
    __syncthreads();
    if (h == 0 && !loader) {
        red[(wr * 64 + col) * 3 + 0] = cnt;
        red[(wr * 64 + col) * 3 + 1] = mean_l;
        red[(wr * 64 + col) * 3 + 2] = m2_l;
    }
    __syncthreads();
    gu64* gbase = (gu64*)(gran + ((size_t)(batch * 8 + nt) * 16) * 64);
    if (tid < 64) {
        float n = red[tid * 3], m = red[tid * 3 + 1], q = red[tid * 3 + 2];
        chan_merge(n, m, q, red[(64 + tid) * 3], red[(64 + tid) * 3 + 1], red[(64 + tid) * 3 + 2]);
        unsigned long long g = ((unsigned long long)(__float_as_uint(q) | 0x80000000u) << 32) | __float_as_uint(m);
        __hip_atomic_store(gbase + mtb * 64 + tid, g, __ATOMIC_RELAXED, __HIP_MEMORY_SCOPE_AGENT);
    }
    float* yt = (float*)(smem + 40960);
    if (!loader) {
#pragma unroll
        for (int mi = 0; mi < 2; ++mi)
#pragma unroll
            for (int i = 0; i < 16; ++i) {
                int row = wr * 64 + mi * 32 + (i & 3) + 8 * (i >> 2) + 4 * h;
                yt[row * 68 + col] = acc[mi][i];
            }
    }
    float* gb = (float*)(smem + 32768);
    if (tid < 64) {
        unsigned long long g[16];
        for (;;) {
            bool ok = true;
#pragma unroll
            for (int j = 0; j < 16; ++j) {
                g[j] = __hip_atomic_load(gbase + j * 64 + tid, __ATOMIC_RELAXED, __HIP_MEMORY_SCOPE_AGENT);
                ok &= (g[j] >> 63) != 0;
            }
            if (__all(ok)) break;
            __builtin_amdgcn_s_sleep(1);
        }
        constexpr float NL = (float)(LL - 15 * BM), INVL = 1.f / (float)LL;
        float sm = 0.f;
#pragma unroll
        for (int j = 0; j < 16; ++j) sm = fmaf(__uint_as_float((unsigned)g[j]), j == 15 ? NL : (float)BM, sm);
        const float m = sm * INVL;
        float q = 0.f;
#pragma unroll
        for (int j = 0; j < 16; ++j) {
            float d = __uint_as_float((unsigned)g[j]) - m;
            q += __uint_as_float((unsigned)(g[j] >> 32) & 0x7fffffffu);
            q = fmaf(d * d, j == 15 ? NL : (float)BM, q);
        }
        float rstd = __builtin_amdgcn_rsqf(fmaf(q, INVL, 1e-5f));
        float ga = gcv * rstd;
        gb[tid] = ga;
        gb[64 + tid] = bcv - m * ga;
    }
    __syncthreads();
    {
        constexpr float K1 = -2.f * 0.7978845608028654f * 1.4426950408889634f;
        constexpr float K2 = K1 * 0.044715f;
        const int ch = tid & 7;
        float ga[8], be[8];
#pragma unroll
        for (int k = 0; k < 2; ++k) {
            float4 t0 = *(const float4*)(gb + ch * 8 + 4 * k);
            float4 t1 = *(const float4*)(gb + 64 + ch * 8 + 4 * k);
            ga[4 * k] = t0.x; ga[4 * k + 1] = t0.y; ga[4 * k + 2] = t0.z; ga[4 * k + 3] = t0.w;
            be[4 * k] = t1.x; be[4 * k + 1] = t1.y; be[4 * k + 2] = t1.z; be[4 * k + 3] = t1.w;
        }
#pragma unroll
        for (int j = 0; j < 2; ++j) {
            const int row = (tid >> 3) + 64 * j;
            float y[8];
#pragma unroll
            for (int k = 0; k < 2; ++k) {
                float4 t = *(const float4*)(yt + row * 68 + ch * 8 + 4 * k);
                y[4 * k] = t.x; y[4 * k + 1] = t.y; y[4 * k + 2] = t.z; y[4 * k + 3] = t.w;
            }
            unsigned pk[4];
#pragma unroll
            for (int k = 0; k < 4; ++k) {
                float o2[2];
#pragma unroll
                for (int u = 0; u < 2; ++u) {
                    float v = fmaf(y[2 * k + u], ga[2 * k + u], be[2 * k + u]);
                    float t = v * fmaf(v * v, K2, K1);
                    float e = __builtin_amdgcn_exp2f(t);
                    o2[u] = v * __builtin_amdgcn_rcpf(1.f + e);
                }
                __hip_bfloat16 lo = __float2bfloat16(o2[0]), hi = __float2bfloat16(o2[1]);
                pk[k] = (unsigned)*(unsigned short*)&lo | ((unsigned)*(unsigned short*)&hi << 16);
            }
            if (row < nvalid)
                *(uint4*)(out + ((size_t)(batch * LL + l0 + row) * DD + n0 + ch * 8)) = make_uint4(pk[0], pk[1], pk[2], pk[3]);
        }
    }
}

__global__ __launch_bounds__(256) void prep_kernel(uint4* __restrict__ flags, const uint4* __restrict__ x, const uint4* __restrict__ w) {
    const int g = blockIdx.x * 256 + threadIdx.x;
    if (g < 8192) flags[g] = make_uint4(0u, 0u, 0u, 0u);
    uint4 a[10];
#pragma unroll
    for (int j = 0; j < 8; ++j) { int i = g + j * 65536; a[j] = x[i < 512000 ? i : 0]; }
    a[8] = w[g];
    a[9] = w[(g & 32767) + 65536];
    unsigned r = 0;
#pragma unroll
    for (int j = 0; j < 10; ++j) r ^= a[j].x ^ a[j].y ^ a[j].z ^ a[j].w;
    asm volatile("" ::"v"(r));
}

extern "C" void kernel_launch(void* const* d_in, const int* in_sizes, int n_in,
                              void* d_out, int out_size, void* d_ws, size_t ws_size,
                              hipStream_t stream) {
    const __hip_bfloat16* x = (const __hip_bfloat16*)d_in[0];
    const __hip_bfloat16* w = (const __hip_bfloat16*)d_in[1];
    const __hip_bfloat16* b = (const __hip_bfloat16*)d_in[2];
    const __hip_bfloat16* gamma = (const __hip_bfloat16*)d_in[3];
    const __hip_bfloat16* beta = (const __hip_bfloat16*)d_in[4];
    __hip_bfloat16* out = (__hip_bfloat16*)d_out;
    unsigned long long* gran = (unsigned long long*)d_ws;
    prep_kernel<<<256, 256, 0, stream>>>((uint4*)d_ws, (const uint4*)x, (const uint4*)w);
    conv_gn_gelu_kernel<<<256, 512, 0, stream>>>(x, w, b, gamma, beta, out, gran);
}
```
